```python
import torch

def _softmax(x, axis=-1):
    return torch.softmax(x, dim=axis)
def _gelu(x):
    return torch.nn.functional.gelu(x, approximate="tanh")
def _silu(x):
    return torch.nn.functional.silu(x)
def _sigmoid(x):
    return torch.sigmoid(x)
def _layernorm(x, axis=-1, eps=1e-5):
    m = x.mean(dim=axis, keepdim=True)
    v = x.var(dim=axis, keepdim=True, unbiased=False)
    return (x - m) / torch.sqrt(v + eps)
def _scatter_add(tgt, idx, src, axis=0):
    return tgt.clone().index_add_(axis, idx, src)
def _segment_sum(src, seg, n):
    out = torch.zeros((n,) + src.shape[1:], dtype=src.dtype)
    return out.index_add_(0, seg, src)
def _pad(x, pw):
    flat = tuple(v for pair in reversed(pw) for v in pair)
    return torch.nn.functional.pad(x, flat)

kernel_name = 'sequential_preLN__sliding_block__dense_activation_squared_relu__prompt'

def setup_inputs():

    _w = (lambda a: (lambda t: t.to(torch.bfloat16) if t.is_floating_point() else t)(torch.as_tensor(a)))
    def _A_setup():
        B, S, D = 1, 2048, 512
        return {
            "x": _w((lambda *s: torch.randn(*s, dtype=torch.bfloat16) * 0.05)(B, S, D)),
            "wqkv": _w((lambda *s: torch.randn(*s, dtype=torch.bfloat16) * 0.05)(D, 3 * D)),
            "wo": _w((lambda *s: torch.randn(*s, dtype=torch.bfloat16) * 0.05)(D, D)),
        }
    _A_in = _A_setup()

    def _B_setup():
        B, S, D, F = 1, 2048, 512, 1376
        return {
            "x": _w((lambda *s: torch.randn(*s, dtype=torch.bfloat16) * 0.05)(B, S, D)),
            "w1": _w((lambda *s: torch.randn(*s, dtype=torch.bfloat16) * 0.05)(D, F)),
            "b1": _w((lambda *s: torch.randn(*s, dtype=torch.bfloat16) * 0.4)(F)),
            "w2": _w((lambda *s: torch.randn(*s, dtype=torch.bfloat16) * 0.05)(F, D)),
            "b2": _w((lambda *s: torch.randn(*s, dtype=torch.bfloat16) * 1.6)(D)),
        }
    _B_in = _B_setup()

    return {
        "A_x": _A_in["x"],
        "A_wqkv": _A_in["wqkv"],
        "A_wo": _A_in["wo"],
        "B_x": _B_in["x"],
        "B_w1": _B_in["w1"],
        "B_b1": _B_in["b1"],
        "B_w2": _B_in["w2"],
        "B_b2": _B_in["b2"],
    }

def reference(A_x, A_wqkv, A_wo, B_x, B_w1, B_b1, B_w2, B_b2):
    def _A_ref(x, wqkv, wo):
        B, S, D = x.shape
        H = 32; Dh = D // H; W = 256
        qkv = (lambda x, *d: x.permute(*d))((x @ wqkv).reshape(B, S, 3, H, Dh), 2, 0, 3, 1, 4)
        q, k, v = qkv[0], qkv[1], qkv[2]
        scores = torch.einsum("bhqd,bhkd->bhqk", q, k) * (Dh ** -0.5)
        idx = torch.arange(S)
        mask = (idx[:, None] - idx[None, :] >= 0) & (idx[:, None] - idx[None, :] < W)
        scores = torch.where(mask[None, None], scores, -1e9)
        p = _softmax(scores, axis=-1)
        o = torch.einsum("bhqk,bhkd->bhqd", p, v)
        return (lambda x, *d: x.permute(*d))(o, 0, 2, 1, 3).reshape(B, S, D) @ wo

    def _B_ref(x, w1, b1, w2, b2):
        h = torch.einsum("bsd,df->bsf", x, w1) + b1
        zero = torch.zeros_like(h)
        r = torch.where(h > zero, h, zero)
        h = r * r
        out = torch.einsum("bsf,fd->bsd", h, w2) + b2
        return out

    x = A_x
    h = x + _A_ref(_layernorm(x, axis=-1), A_wqkv, A_wo)
    h2 = h + _B_ref(_layernorm(h, axis=-1), B_w1, B_b1, B_w2, B_b2)
    return h2
```

```cpp
#include <hip/hip_runtime.h>
#include <hip/hip_bf16.h>
typedef unsigned short u16;
__device__ __forceinline__ float bf2f(u16 b){ return __uint_as_float(((unsigned)b)<<16); }
__device__ __forceinline__ u16 f2bf(float f){ unsigned u=__float_as_uint(f); u += 0x7FFFu + ((u>>16)&1u); return (u16)(u>>16); }
constexpr int S=2048,D=512,F=1376,H=32,DH=16,W=256;
__global__ void ln_k(const u16* x, u16* y){
  int r=blockIdx.x; int t=threadIdx.x; __shared__ float red[256];
  float a=bf2f(x[r*D+t]), b=bf2f(x[r*D+256+t]);
  red[t]=a+b; __syncthreads();
  for(int s=128;s>0;s>>=1){ if(t<s) red[t]+=red[t+s]; __syncthreads(); }
  float m=red[0]/D; __syncthreads();
  red[t]=(a-m)*(a-m)+(b-m)*(b-m); __syncthreads();
  for(int s=128;s>0;s>>=1){ if(t<s) red[t]+=red[t+s]; __syncthreads(); }
  float rs=rsqrtf(red[0]/D+1e-5f);
  y[r*D+t]=f2bf((a-m)*rs); y[r*D+256+t]=f2bf((b-m)*rs);
}
__global__ void gemm_k(const u16* A,const u16* B,u16* C,int M,int N,int K,int mode,const u16* bias,const u16* res){
  __shared__ float As[16][17], Bs[16][17];
  int tx=threadIdx.x&15, ty=threadIdx.x>>4; int row=blockIdx.y*16+ty, col=blockIdx.x*16+tx;
  float acc=0;
  for(int k0=0;k0<K;k0+=16){
    As[ty][tx]=bf2f(A[row*K+k0+tx]); Bs[ty][tx]=bf2f(B[(k0+ty)*N+col]); __syncthreads();
    for(int k=0;k<16;k++) acc+=As[ty][k]*Bs[k][tx];
    __syncthreads();
  }
  if(mode==1) acc=acc+bf2f(res[row*N+col]);
  if(mode==2){ acc+=bf2f(bias[col]); acc=acc>0?acc*acc:0; }
  if(mode==3) acc=acc+bf2f(bias[col])+bf2f(res[row*N+col]);
  C[row*N+col]=f2bf(acc);
}
__global__ void attn_k(const u16* qkv,u16* o){
  int idx=blockIdx.x*blockDim.x+threadIdx.x; int h=idx&31, q=idx>>5;
  float qv[16];
  #pragma unroll
  for(int d=0;d<16;d++) qv[d]=bf2f(qkv[q*1536+h*16+d])*0.25f;
  float m=-1e30f,l=0,acc[16];
  #pragma unroll
  for(int d=0;d<16;d++) acc[d]=0;
  int k0=q-255; if(k0<0)k0=0;
  for(int k=k0;k<=q;k++){
    float s=0;
    #pragma unroll
    for(int d=0;d<16;d++) s+=qv[d]*bf2f(qkv[k*1536+512+h*16+d]);
    float mn=fmaxf(m,s); float a=__expf(m-mn), p=__expf(s-mn); l=l*a+p;
    #pragma unroll
    for(int d=0;d<16;d++) acc[d]=acc[d]*a+p*bf2f(qkv[k*1536+1024+h*16+d]);
    m=mn;
  }
  #pragma unroll
  for(int d=0;d<16;d++) o[q*512+h*16+d]=f2bf(acc[d]/l);
}
extern "C" void kernel_launch(void* const* d_in, const int* in_sizes, int n_in, void* d_out, int out_size, void* d_ws, size_t ws_size, hipStream_t stream){
  const u16* x=(const u16*)d_in[0]; const u16* wqkv=(const u16*)d_in[1]; const u16* wo=(const u16*)d_in[2];
  const u16* w1=(const u16*)d_in[4]; const u16* b1=(const u16*)d_in[5]; const u16* w2=(const u16*)d_in[6]; const u16* b2=(const u16*)d_in[7];
  u16* ws=(u16*)d_ws; u16* ln=ws; u16* qkv=ln+S*D; u16* o=qkv+S*1536; u16* h=o+S*D; u16* hid=h+S*D;
  ln_k<<<S,256,0,stream>>>(x,ln);
  gemm_k<<<dim3(1536/16,S/16),256,0,stream>>>(ln,wqkv,qkv,S,1536,D,0,nullptr,nullptr);
  attn_k<<<S*H/256,256,0,stream>>>(qkv,o);
  gemm_k<<<dim3(D/16,S/16),256,0,stream>>>(o,wo,h,S,D,D,1,nullptr,x);
  ln_k<<<S,256,0,stream>>>(h,ln);
  gemm_k<<<dim3(F/16,S/16),256,0,stream>>>(ln,w1,hid,S,F,D,2,b1,nullptr);
  gemm_k<<<dim3(D/16,S/16),256,0,stream>>>(hid,w2,(u16*)d_out,S,D,F,3,b2,h);
}
```

```cpp
#include <hip/hip_runtime.h>
#include <hip/hip_bf16.h>
typedef unsigned short u16;
__device__ __forceinline__ float bf2f(u16 b){ return __uint_as_float(((unsigned)b)<<16); }
__device__ __forceinline__ u16 f2bf(float f){ unsigned u=__float_as_uint(f); u += 0x7FFFu + ((u>>16)&1u); return (u16)(u>>16); }
constexpr int S=2048,D=512,F=1376,H=32,DH=16,W=256;
__global__ void ln_k(const u16* x, u16* y){
  int r=blockIdx.x; int t=threadIdx.x; __shared__ float red[256];
  float a=bf2f(x[r*D+t]), b=bf2f(x[r*D+256+t]);
  red[t]=a+b; __syncthreads();
  for(int s=128;s>0;s>>=1){ if(t<s) red[t]+=red[t+s]; __syncthreads(); }
  float m=red[0]/D; __syncthreads();
  red[t]=(a-m)*(a-m)+(b-m)*(b-m); __syncthreads();
  for(int s=128;s>0;s>>=1){ if(t<s) red[t]+=red[t+s]; __syncthreads(); }
  float rs=rsqrtf(red[0]/D+1e-5f);
  y[r*D+t]=f2bf((a-m)*rs); y[r*D+256+t]=f2bf((b-m)*rs);
}
__global__ void gemm_k(const u16* A,const u16* B,u16* C,int M,int N,int K,int mode,const u16* bias,const u16* res){
  __shared__ float As[16][17], Bs[16][17];
  int tx=threadIdx.x&15, ty=threadIdx.x>>4; int row=blockIdx.y*16+ty, col=blockIdx.x*16+tx;
  float acc=0;
  for(int k0=0;k0<K;k0+=16){
    As[ty][tx]=bf2f(A[row*K+k0+tx]); Bs[ty][tx]=bf2f(B[(k0+ty)*N+col]); __syncthreads();
    for(int k=0;k<16;k++) acc+=As[ty][k]*Bs[k][tx];
    __syncthreads();
  }
  if(mode==1) acc=acc+bf2f(res[row*N+col]);
  if(mode==2){ acc+=bf2f(bias[col]); acc=acc>0?acc*acc:0; }
  if(mode==3) acc=acc+bf2f(bias[col])+bf2f(res[row*N+col]);
  C[row*N+col]=f2bf(acc);
}
__global__ void attn_k(const u16* qkv,u16* o){
  int idx=blockIdx.x*blockDim.x+threadIdx.x; int h=idx&31, q=idx>>5;
  float qv[16];
  #pragma unroll
  for(int d=0;d<16;d++) qv[d]=bf2f(qkv[q*1536+h*16+d])*0.25f;
  float m=-1e30f,l=0,acc[16];
  #pragma unroll
  for(int d=0;d<16;d++) acc[d]=0;
  int k0=q-255; if(k0<0)k0=0;
  for(int k=k0;k<=q;k++){
    float s=0;
    #pragma unroll
    for(int d=0;d<16;d++) s+=qv[d]*bf2f(qkv[k*1536+512+h*16+d]);
    float mn=fmaxf(m,s); float a=__expf(m-mn), p=__expf(s-mn); l=l*a+p;
    #pragma unroll
    for(int d=0;d<16;d++) acc[d]=acc[d]*a+p*bf2f(qkv[k*1536+1024+h*16+d]);
    m=mn;
  }
  #pragma unroll
  for(int d=0;d<16;d++) o[q*512+h*16+d]=f2bf(acc[d]/l);
}
#define SELB (0)

typedef unsigned v4u __attribute__((ext_vector_type(4)));
#define GAS __attribute__((address_space(1)))
__device__ __forceinline__ void glds16(const void* g, unsigned lds_base) {
    asm volatile("s_mov_b32 m0, %1\n\ts_nop 0\n\tglobal_load_lds_dwordx4 %0, off" :: "v"(g), "s"(lds_base) : "memory"); }
__device__ __forceinline__ unsigned long long mt(){ unsigned long long t; asm volatile("s_memtime %0\n\ts_waitcnt lgkmcnt(0)":"=s"(t)::"memory"); return t; }
template<int MODE,int MAP,int ROT>
__global__ void __launch_bounds__(256,1) probe_k(const char* x, const char* w, const char* w1, char* ws){
  extern __shared__ __attribute__((aligned(16))) char lds[];
  unsigned long long c0=mt();
  int tid=threadIdx.x, lane=tid&63; int wv=__builtin_amdgcn_readfirstlane(tid>>6);
  unsigned long long T=0;
  if(MODE==0){ T=0; }
  if(MODE==1){
    int xg=blockIdx.x&7, j=blockIdx.x>>3; int rb,hg;
    if(MAP==0){ rb=xg*4+(j>>3); hg=j&7; } else { rb=(xg>>1)*8+(j>>2); hg=(xg&1)*4+(j&3); }
    int rot = ROT? (MAP==0 ? (2*rb+hg)&7 : (rb+2*hg)&7) : 0;
    unsigned l0=(unsigned)(uintptr_t)lds;
    const char* xb=x+(size_t)(rb*64+wv*16+(lane>>3))*1024+(lane&7)*16;
    const char* wb=w+(size_t)(wv*16+(lane>>3))*3072+hg*128+(lane&7)*16;
    auto XC=[&](int c){ int cc=(c+rot)&7; for(int i=0;i<2;i++) glds16(xb+i*8192+cc*128, l0+c*8192+wv*2048+i*1024); };
    auto WC=[&](int c){ int cc=(c+rot)&7; for(int i=0;i<6;i++) glds16(wb+(size_t)cc*64*3072+(i&1)*8*3072+(i>>1)*1024, l0+65536+(c&3)*24576+wv*6144+i*1024); };
    #pragma unroll
    for(int c=0;c<4;c++){ XC(c); WC(c); }
    #pragma unroll
    for(int c=4;c<8;c++) XC(c);
    asm volatile("s_waitcnt vmcnt(30)":::"memory");
    for(int c=4;c<8;c++) WC(c);
    asm volatile("s_waitcnt vmcnt(0)\n\ts_barrier":::"memory");
    T=mt()-c0;
  }
  if(MODE==2){
    if(blockIdx.x==0){
      unsigned a=*(const GAS unsigned*)(x+lane*4); asm volatile("s_waitcnt vmcnt(0)"::"v"(a):"memory");
      unsigned long long t1=mt();
      unsigned b=*(const GAS unsigned*)(x+65536+4096*3+lane*4+(a&0)); asm volatile("s_waitcnt vmcnt(0)"::"v"(b):"memory");
      T=mt()-t1; }
  }
  if(MODE==3){
    if(blockIdx.x==0){
      unsigned long long t1=mt();
      unsigned a=*(const GAS unsigned*)(x+lane*4); asm volatile("s_waitcnt vmcnt(0)"::"v"(a):"memory");
      T=mt()-t1; }
  }
  if(MODE==4){
    unsigned long long t1=mt();
    asm volatile(".rept 2048\n\tv_max3_f32 v20, v21, v22, v23\n\t.endr":::"v20");
    T=mt()-t1;
  }
  if(blockIdx.x==SELB || SELB<0){
    unsigned long long r0=__builtin_amdgcn_s_memrealtime();
    while(__builtin_amdgcn_s_memrealtime()-r0 < T*2) __builtin_amdgcn_s_sleep(8);
  }
}

extern "C" void kernel_launch(void* const* d_in, const int* in_sizes, int n_in, void* d_out, int out_size, void* d_ws, size_t ws_size, hipStream_t stream){
  const u16* x=(const u16*)d_in[0]; const u16* wqkv=(const u16*)d_in[1]; const u16* wo=(const u16*)d_in[2];
  const u16* w1=(const u16*)d_in[4]; const u16* b1=(const u16*)d_in[5]; const u16* w2=(const u16*)d_in[6]; const u16* b2=(const u16*)d_in[7];
  u16* ws=(u16*)d_ws; u16* ln=ws; u16* qkv=ln+S*D; u16* o=qkv+S*1536; u16* h=o+S*D; u16* hid=h+S*D;
  hipFuncSetAttribute((const void*)probe_k<0,0,0>, hipFuncAttributeMaxDynamicSharedMemorySize, 160*1024);
  probe_k<0,0,0><<<256,256,160*1024,stream>>>((const char*)x,(const char*)wqkv,(const char*)w1,(char*)d_ws);
  ln_k<<<S,256,0,stream>>>(x,ln);
  gemm_k<<<dim3(1536/16,S/16),256,0,stream>>>(ln,wqkv,qkv,S,1536,D,0,nullptr,nullptr);
  attn_k<<<S*H/256,256,0,stream>>>(qkv,o);
  gemm_k<<<dim3(D/16,S/16),256,0,stream>>>(o,wo,h,S,D,D,1,nullptr,x);
  ln_k<<<S,256,0,stream>>>(h,ln);
  gemm_k<<<dim3(F/16,S/16),256,0,stream>>>(ln,w1,hid,S,F,D,2,b1,nullptr);
  gemm_k<<<dim3(D/16,S/16),256,0,stream>>>(hid,w2,(u16*)d_out,S,D,F,3,b2,h);
}
```

```cpp
#include <hip/hip_runtime.h>
#include <hip/hip_bf16.h>
typedef unsigned short u16;
__device__ __forceinline__ float bf2f(u16 b){ return __uint_as_float(((unsigned)b)<<16); }
__device__ __forceinline__ u16 f2bf(float f){ unsigned u=__float_as_uint(f); u += 0x7FFFu + ((u>>16)&1u); return (u16)(u>>16); }
constexpr int S=2048,D=512,F=1376,H=32,DH=16,W=256;
__global__ void ln_k(const u16* x, u16* y){
  int r=blockIdx.x; int t=threadIdx.x; __shared__ float red[256];
  float a=bf2f(x[r*D+t]), b=bf2f(x[r*D+256+t]);
  red[t]=a+b; __syncthreads();
  for(int s=128;s>0;s>>=1){ if(t<s) red[t]+=red[t+s]; __syncthreads(); }
  float m=red[0]/D; __syncthreads();
  red[t]=(a-m)*(a-m)+(b-m)*(b-m); __syncthreads();
  for(int s=128;s>0;s>>=1){ if(t<s) red[t]+=red[t+s]; __syncthreads(); }
  float rs=rsqrtf(red[0]/D+1e-5f);
  y[r*D+t]=f2bf((a-m)*rs); y[r*D+256+t]=f2bf((b-m)*rs);
}
__global__ void gemm_k(const u16* A,const u16* B,u16* C,int M,int N,int K,int mode,const u16* bias,const u16* res){
  __shared__ float As[16][17], Bs[16][17];
  int tx=threadIdx.x&15, ty=threadIdx.x>>4; int row=blockIdx.y*16+ty, col=blockIdx.x*16+tx;
  float acc=0;
  for(int k0=0;k0<K;k0+=16){
    As[ty][tx]=bf2f(A[row*K+k0+tx]); Bs[ty][tx]=bf2f(B[(k0+ty)*N+col]); __syncthreads();
    for(int k=0;k<16;k++) acc+=As[ty][k]*Bs[k][tx];
    __syncthreads();
  }
  if(mode==1) acc=acc+bf2f(res[row*N+col]);
  if(mode==2){ acc+=bf2f(bias[col]); acc=acc>0?acc*acc:0; }
  if(mode==3) acc=acc+bf2f(bias[col])+bf2f(res[row*N+col]);
  C[row*N+col]=f2bf(acc);
}
__global__ void attn_k(const u16* qkv,u16* o){
  int idx=blockIdx.x*blockDim.x+threadIdx.x; int h=idx&31, q=idx>>5;
  float qv[16];
  #pragma unroll
  for(int d=0;d<16;d++) qv[d]=bf2f(qkv[q*1536+h*16+d])*0.25f;
  float m=-1e30f,l=0,acc[16];
  #pragma unroll
  for(int d=0;d<16;d++) acc[d]=0;
  int k0=q-255; if(k0<0)k0=0;
  for(int k=k0;k<=q;k++){
    float s=0;
    #pragma unroll
    for(int d=0;d<16;d++) s+=qv[d]*bf2f(qkv[k*1536+512+h*16+d]);
    float mn=fmaxf(m,s); float a=__expf(m-mn), p=__expf(s-mn); l=l*a+p;
    #pragma unroll
    for(int d=0;d<16;d++) acc[d]=acc[d]*a+p*bf2f(qkv[k*1536+1024+h*16+d]);
    m=mn;
  }
  #pragma unroll
  for(int d=0;d<16;d++) o[q*512+h*16+d]=f2bf(acc[d]/l);
}
__global__ void first_k(unsigned long long* t){ if(blockIdx.x==0 && threadIdx.x==0) t[0]=__builtin_amdgcn_s_memrealtime(); }
__global__ void last_k(const unsigned long long* t){ unsigned long long t0=t[0]; for(int i=0;i<100000000;i++){ unsigned long long d=__builtin_amdgcn_s_memrealtime()-t0; if(d>=100000ull) break; } }
extern "C" void kernel_launch(void* const* d_in, const int* in_sizes, int n_in, void* d_out, int out_size, void* d_ws, size_t ws_size, hipStream_t stream){
  const u16* x=(const u16*)d_in[0]; const u16* wqkv=(const u16*)d_in[1]; const u16* wo=(const u16*)d_in[2];
  const u16* w1=(const u16*)d_in[4]; const u16* b1=(const u16*)d_in[5]; const u16* w2=(const u16*)d_in[6]; const u16* b2=(const u16*)d_in[7];
  u16* ws=(u16*)d_ws; u16* ln=ws; u16* qkv=ln+S*D; u16* o=qkv+S*1536; u16* h=o+S*D; u16* hid=h+S*D;
  first_k<<<1,64,0,stream>>>((unsigned long long*)((char*)d_ws+(64<<20)));
  ln_k<<<S,256,0,stream>>>(x,ln);
  gemm_k<<<dim3(1536/16,S/16),256,0,stream>>>(ln,wqkv,qkv,S,1536,D,0,nullptr,nullptr);
  attn_k<<<S*H/256,256,0,stream>>>(qkv,o);
  gemm_k<<<dim3(D/16,S/16),256,0,stream>>>(o,wo,h,S,D,D,1,nullptr,x);
  ln_k<<<S,256,0,stream>>>(h,ln);
  gemm_k<<<dim3(F/16,S/16),256,0,stream>>>(ln,w1,hid,S,F,D,2,b1,nullptr);
  gemm_k<<<dim3(D/16,S/16),256,0,stream>>>(hid,w2,(u16*)d_out,S,D,F,3,b2,h);
  last_k<<<1,64,0,stream>>>((const unsigned long long*)((char*)d_ws+(64<<20)));
}
```

```cpp
#include <hip/hip_runtime.h>
#include <hip/hip_bf16.h>
typedef unsigned short u16;
__device__ __forceinline__ float bf2f(u16 b){ return __uint_as_float(((unsigned)b)<<16); }
__device__ __forceinline__ u16 f2bf(float f){ unsigned u=__float_as_uint(f); u += 0x7FFFu + ((u>>16)&1u); return (u16)(u>>16); }
constexpr int S=2048,D=512,F=1376,H=32,DH=16,W=256;
__global__ void ln_k(const u16* x, u16* y){
  int r=blockIdx.x; int t=threadIdx.x; __shared__ float red[256];
  float a=bf2f(x[r*D+t]), b=bf2f(x[r*D+256+t]);
  red[t]=a+b; __syncthreads();
  for(int s=128;s>0;s>>=1){ if(t<s) red[t]+=red[t+s]; __syncthreads(); }
  float m=red[0]/D; __syncthreads();
  red[t]=(a-m)*(a-m)+(b-m)*(b-m); __syncthreads();
  for(int s=128;s>0;s>>=1){ if(t<s) red[t]+=red[t+s]; __syncthreads(); }
  float rs=rsqrtf(red[0]/D+1e-5f);
  y[r*D+t]=f2bf((a-m)*rs); y[r*D+256+t]=f2bf((b-m)*rs);
}
__global__ void gemm_k(const u16* A,const u16* B,u16* C,int M,int N,int K,int mode,const u16* bias,const u16* res){
  __shared__ float As[16][17], Bs[16][17];
  int tx=threadIdx.x&15, ty=threadIdx.x>>4; int row=blockIdx.y*16+ty, col=blockIdx.x*16+tx;
  float acc=0;
  for(int k0=0;k0<K;k0+=16){
    As[ty][tx]=bf2f(A[row*K+k0+tx]); Bs[ty][tx]=bf2f(B[(k0+ty)*N+col]); __syncthreads();
    for(int k=0;k<16;k++) acc+=As[ty][k]*Bs[k][tx];
    __syncthreads();
  }
  if(mode==1) acc=acc+bf2f(res[row*N+col]);
  if(mode==2){ acc+=bf2f(bias[col]); acc=acc>0?acc*acc:0; }
  if(mode==3) acc=acc+bf2f(bias[col])+bf2f(res[row*N+col]);
  C[row*N+col]=f2bf(acc);
}
__global__ void attn_k(const u16* qkv,u16* o){
  int idx=blockIdx.x*blockDim.x+threadIdx.x; int h=idx&31, q=idx>>5;
  float qv[16];
  #pragma unroll
  for(int d=0;d<16;d++) qv[d]=bf2f(qkv[q*1536+h*16+d])*0.25f;
  float m=-1e30f,l=0,acc[16];
  #pragma unroll
  for(int d=0;d<16;d++) acc[d]=0;
  int k0=q-255; if(k0<0)k0=0;
  for(int k=k0;k<=q;k++){
    float s=0;
    #pragma unroll
    for(int d=0;d<16;d++) s+=qv[d]*bf2f(qkv[k*1536+512+h*16+d]);
    float mn=fmaxf(m,s); float a=__expf(m-mn), p=__expf(s-mn); l=l*a+p;
    #pragma unroll
    for(int d=0;d<16;d++) acc[d]=acc[d]*a+p*bf2f(qkv[k*1536+1024+h*16+d]);
    m=mn;
  }
  #pragma unroll
  for(int d=0;d<16;d++) o[q*512+h*16+d]=f2bf(acc[d]/l);
}
__global__ void first_k(unsigned long long* t, const char* p){ unsigned long long r=__builtin_amdgcn_s_memrealtime();
  typedef __attribute__((address_space(1))) const unsigned gu;
  unsigned a=*(gu*)(p+threadIdx.x*4); asm volatile("s_waitcnt vmcnt(0)"::"v"(a):"memory");
  unsigned long long c0; asm volatile("s_memtime %0\n\ts_waitcnt lgkmcnt(0)":"=s"(c0)::"memory");
  for(int i=1;i<=16;i++){ unsigned b=*(gu*)(p+i*(4096*29+256)+threadIdx.x*4); asm volatile("s_waitcnt vmcnt(0)"::"v"(b):"memory"); }
  unsigned long long c1; asm volatile("s_memtime %0\n\ts_waitcnt lgkmcnt(0)":"=s"(c1)::"memory");
  if(threadIdx.x==0) t[0]=r+(c1-c0)*10/16; }
__global__ void last_k(const unsigned long long* t){ unsigned long long t0=t[0]; for(int i=0;i<100000000;i++){ long long d=(long long)(__builtin_amdgcn_s_memrealtime()-t0); if(d>=100000ll) break; } }
extern "C" void kernel_launch(void* const* d_in, const int* in_sizes, int n_in, void* d_out, int out_size, void* d_ws, size_t ws_size, hipStream_t stream){
  const u16* x=(const u16*)d_in[0]; const u16* wqkv=(const u16*)d_in[1]; const u16* wo=(const u16*)d_in[2];
  const u16* w1=(const u16*)d_in[4]; const u16* b1=(const u16*)d_in[5]; const u16* w2=(const u16*)d_in[6]; const u16* b2=(const u16*)d_in[7];
  u16* ws=(u16*)d_ws; u16* ln=ws; u16* qkv=ln+S*D; u16* o=qkv+S*1536; u16* h=o+S*D; u16* hid=h+S*D;
  first_k<<<1,64,0,stream>>>((unsigned long long*)((char*)d_ws+(64<<20)), (const char*)d_ws+(128<<20));
  ln_k<<<S,256,0,stream>>>(x,ln);
  gemm_k<<<dim3(1536/16,S/16),256,0,stream>>>(ln,wqkv,qkv,S,1536,D,0,nullptr,nullptr);
  attn_k<<<S*H/256,256,0,stream>>>(qkv,o);
  gemm_k<<<dim3(D/16,S/16),256,0,stream>>>(o,wo,h,S,D,D,1,nullptr,x);
  ln_k<<<S,256,0,stream>>>(h,ln);
  gemm_k<<<dim3(F/16,S/16),256,0,stream>>>(ln,w1,hid,S,F,D,2,b1,nullptr);
  gemm_k<<<dim3(D/16,S/16),256,0,stream>>>(hid,w2,(u16*)d_out,S,D,F,3,b2,h);
  last_k<<<1,64,0,stream>>>((const unsigned long long*)((char*)d_ws+(64<<20)));
}
```

```cpp
#include <hip/hip_runtime.h>
#include <hip/hip_bf16.h>
typedef unsigned short u16;
__device__ __forceinline__ float bf2f(u16 b){ return __uint_as_float(((unsigned)b)<<16); }
__device__ __forceinline__ u16 f2bf(float f){ unsigned u=__float_as_uint(f); u += 0x7FFFu + ((u>>16)&1u); return (u16)(u>>16); }
constexpr int S=2048,D=512,F=1376,H=32,DH=16,W=256;
__global__ void ln_k(const u16* x, u16* y){
  int r=blockIdx.x; int t=threadIdx.x; __shared__ float red[256];
  float a=bf2f(x[r*D+t]), b=bf2f(x[r*D+256+t]);
  red[t]=a+b; __syncthreads();
  for(int s=128;s>0;s>>=1){ if(t<s) red[t]+=red[t+s]; __syncthreads(); }
  float m=red[0]/D; __syncthreads();
  red[t]=(a-m)*(a-m)+(b-m)*(b-m); __syncthreads();
  for(int s=128;s>0;s>>=1){ if(t<s) red[t]+=red[t+s]; __syncthreads(); }
  float rs=rsqrtf(red[0]/D+1e-5f);
  y[r*D+t]=f2bf((a-m)*rs); y[r*D+256+t]=f2bf((b-m)*rs);
}
__global__ void gemm_k(const u16* A,const u16* B,u16* C,int M,int N,int K,int mode,const u16* bias,const u16* res){
  __shared__ float As[16][17], Bs[16][17];
  int tx=threadIdx.x&15, ty=threadIdx.x>>4; int row=blockIdx.y*16+ty, col=blockIdx.x*16+tx;
  float acc=0;
  for(int k0=0;k0<K;k0+=16){
    As[ty][tx]=bf2f(A[row*K+k0+tx]); Bs[ty][tx]=bf2f(B[(k0+ty)*N+col]); __syncthreads();
    for(int k=0;k<16;k++) acc+=As[ty][k]*Bs[k][tx];
    __syncthreads();
  }
  if(mode==1) acc=acc+bf2f(res[row*N+col]);
  if(mode==2){ acc+=bf2f(bias[col]); acc=acc>0?acc*acc:0; }
  if(mode==3) acc=acc+bf2f(bias[col])+bf2f(res[row*N+col]);
  C[row*N+col]=f2bf(acc);
}
__global__ void attn_k(const u16* qkv,u16* o){
  int idx=blockIdx.x*blockDim.x+threadIdx.x; int h=idx&31, q=idx>>5;
  float qv[16];
  #pragma unroll
  for(int d=0;d<16;d++) qv[d]=bf2f(qkv[q*1536+h*16+d])*0.25f;
  float m=-1e30f,l=0,acc[16];
  #pragma unroll
  for(int d=0;d<16;d++) acc[d]=0;
  int k0=q-255; if(k0<0)k0=0;
  for(int k=k0;k<=q;k++){
    float s=0;
    #pragma unroll
    for(int d=0;d<16;d++) s+=qv[d]*bf2f(qkv[k*1536+512+h*16+d]);
    float mn=fmaxf(m,s); float a=__expf(m-mn), p=__expf(s-mn); l=l*a+p;
    #pragma unroll
    for(int d=0;d<16;d++) acc[d]=acc[d]*a+p*bf2f(qkv[k*1536+1024+h*16+d]);
    m=mn;
  }
  #pragma unroll
  for(int d=0;d<16;d++) o[q*512+h*16+d]=f2bf(acc[d]/l);
}
__device__ __forceinline__ unsigned at(char* p){ unsigned v; unsigned z=0; asm volatile("global_atomic_add %0, %1, %2, off sc0 sc1\n\ts_waitcnt vmcnt(0)":"=&v"(v):"v"(p),"v"(z):"memory"); return v; }
__global__ void first_k(unsigned long long* t, char* p0){ unsigned long long r=__builtin_amdgcn_s_memrealtime();
  unsigned xcc=(unsigned)__builtin_amdgcn_s_getreg((3<<11)|20)&0xF;
  unsigned long long code=0;
  for(int i=0;i<4;i++){
    uintptr_t a=((uintptr_t)p0+(size_t)i*(6<<20)+16383)&~(uintptr_t)16383; unsigned par=((a>>21)&1)^(xcc>>2); char* p=(char*)(a+par*8192);
    at(p); at(p);
    unsigned long long c0,c1; asm volatile("s_memtime %0\n\ts_waitcnt lgkmcnt(0)":"=s"(c0)::"memory"); at(p); asm volatile("s_memtime %0\n\ts_waitcnt lgkmcnt(0)":"=s"(c1)::"memory");
    if(c1-c0>670) code|=1ull<<i; }
  t[0]=r+code*1000+xcc*16000; }
__global__ void last_k(const unsigned long long* t){ unsigned long long t0=t[0]; for(int i=0;i<100000000;i++){ long long d=(long long)(__builtin_amdgcn_s_memrealtime()-t0); if(d>=100000ll) break; } }
extern "C" void kernel_launch(void* const* d_in, const int* in_sizes, int n_in, void* d_out, int out_size, void* d_ws, size_t ws_size, hipStream_t stream){
  const u16* x=(const u16*)d_in[0]; const u16* wqkv=(const u16*)d_in[1]; const u16* wo=(const u16*)d_in[2];
  const u16* w1=(const u16*)d_in[4]; const u16* b1=(const u16*)d_in[5]; const u16* w2=(const u16*)d_in[6]; const u16* b2=(const u16*)d_in[7];
  u16* ws=(u16*)d_ws; u16* ln=ws; u16* qkv=ln+S*D; u16* o=qkv+S*1536; u16* h=o+S*D; u16* hid=h+S*D;
  first_k<<<1,1,0,stream>>>((unsigned long long*)((char*)d_ws+(64<<20)), (char*)d_ws+(176<<20));
  ln_k<<<S,256,0,stream>>>(x,ln);
  gemm_k<<<dim3(1536/16,S/16),256,0,stream>>>(ln,wqkv,qkv,S,1536,D,0,nullptr,nullptr);
  attn_k<<<S*H/256,256,0,stream>>>(qkv,o);
  gemm_k<<<dim3(D/16,S/16),256,0,stream>>>(o,wo,h,S,D,D,1,nullptr,x);
  ln_k<<<S,256,0,stream>>>(h,ln);
  gemm_k<<<dim3(F/16,S/16),256,0,stream>>>(ln,w1,hid,S,F,D,2,b1,nullptr);
  gemm_k<<<dim3(D/16,S/16),256,0,stream>>>(hid,w2,(u16*)d_out,S,D,F,3,b2,h);
  last_k<<<1,64,0,stream>>>((const unsigned long long*)((char*)d_ws+(64<<20)));
}
```

```cpp
#include <hip/hip_runtime.h>

__global__ void __launch_bounds__(256, 1) block_fwd(const void* x, const void* wqkv, const void* wo, const void* w1, const void* b1,
                                                    const void* w2, const void* b2, void* out, void* ws) {
  asm volatile(
    "L_begin_%=:\n"
    "s_load_dwordx16 s[40:55], %0, 0x0\n"
    "s_load_dwordx2 s[56:57], %0, 0x40\n"
    "v_and_b32 v34, 63, %2\n"
    "v_lshrrev_b32 v36, 6, %2\n"
    "s_and_b32 s88, %1, 7\n"
    "s_lshr_b32 s89, %1, 3\n"
    "v_readfirstlane_b32 s74, v36\n"
    "s_getpc_b64 s[92:93]\n"
    "L_pc_%=:\n"
    "s_lshl_b32 s91, s89, 2\n"
    "s_add_u32 s91, s91, s74\n"
    "s_lshl_b32 s91, s91, 8\n"
    "v_and_b32 v37, 1, v34\n"
    "v_lshl_add_u32 v37, v37, 7, s91\n"
    "v_min_u32 v37, L_end_%=-L_pc_%=-4, v37\n"
    "global_load_dword v16, v37, s[92:93]\n"
    "s_lshr_b32 s77, s89, 2\n"
    "s_lshr_b32 s75, s88, 1\n"
    "s_lshl_b32 s75, s75, 3\n"
    "s_add_u32 s75, s75, s77\n"
    "s_and_b32 s76, s88, 1\n"
    "s_lshl_b32 s76, s76, 2\n"
    "s_and_b32 s90, s89, 3\n"
    "s_add_u32 s76, s76, s90\n"
    "s_lshl_b32 s90, s90, 1\n"
    "s_add_u32 s78, s77, s90\n"
    "s_and_b32 s78, s78, 7\n"
    "s_lshl_b32 s84, s74, 12\n"
    "s_lshl_b32 s85, s74, 11\n"
    "s_lshl_b32 s86, s74, 10\n"
    "v_lshrrev_b32 v36, 3, v34\n"
    "v_and_b32 v37, 7, v34\n"
    "s_lshl_b32 s88, s74, 14\n"
    "s_lshl_b32 s94, s75, 16\n"
    "v_lshrrev_b32 v38, 1, v36\n"
    "v_xor_b32 v38, v38, v37\n"
    "v_lshlrev_b32 v38, 4, v38\n"
    "v_lshl_add_u32 v72, v36, 10, v38\n"
    "v_add_u32 v72, s88, v72\n"
    "v_xor_b32 v73, 64, v72\n"
    "v_add_u32 v73, 0x1c00, v73\n"
    "s_mul_i32 s88, s74, 49152\n"
    "s_lshl_b32 s89, s76, 7\n"
    "s_add_u32 s88, s88, s89\n"
    "v_and_b32 v38, 2, v36\n"
    "v_lshlrev_b32 v38, 1, v38\n"
    "v_xor_b32 v38, v38, v37\n"
    "v_lshlrev_b32 v38, 4, v38\n"
    "v_mul_u32_u24 v74, 3072, v36\n"
    "v_add3_u32 v74, v74, v38, s88\n"
    "v_xor_b32 v75, 32, v74\n"
    "v_add_u32 v75, 23552, v75\n"
    "v_add_u32 v76, 1024, v74\n"
    "s_add_u32 s88, s78, 0\n"
    "s_and_b32 s88, s88, 7\n"
    "s_lshl_b32 s58, s88, 7\n"
    "s_add_u32 s58, s58, s94\n"
    "s_mul_i32 s66, s88, 196608\n"
    "s_add_u32 s88, s78, 1\n"
    "s_and_b32 s88, s88, 7\n"
    "s_lshl_b32 s59, s88, 7\n"
    "s_add_u32 s59, s59, s94\n"
    "s_mul_i32 s67, s88, 196608\n"
    "s_mov_b32 s10, 0x200000\n"
    "s_mov_b32 s11, 0x20000\n"
    "s_mov_b32 s14, 0x180000\n"
    "s_mov_b32 s15, 0x20000\n"
    "s_waitcnt lgkmcnt(0)\n"
    "s_mov_b32 s8, s40\n"
    "s_and_b32 s9, s41, 0xffff\n"
    "s_mov_b32 s12, s42\n"
    "s_and_b32 s13, s43, 0xffff\n"
    "s_add_u32 s56, s56, 0xfffff\n"
    "s_addc_u32 s57, s57, 0\n"
    "s_and_b32 s56, s56, 0xfff00000\n"
    "s_and_b32 s88, %1, 0xe6\n"
    "s_or_b32 s88, s88, s74\n"
    "s_cbranch_scc0 L_pa_%=\n"
    "L_pad_%=:\n"
    "s_add_u32 m0, s85, 0x1a000\n"
    "s_nop 0\n"
    "buffer_load_dwordx4 v72, s[8:11], s58 offen lds\n"
    "buffer_load_dwordx4 v73, s[8:11], s58 offen offset:1024 lds\n"
    "s_add_u32 m0, s84, 0x0\n"
    "s_nop 0\n"
    "buffer_load_dwordx4 v76, s[12:15], s66 offen lds\n"
    "buffer_load_dwordx4 v76, s[12:15], s66 offen offset:1024 lds\n"
    "buffer_load_dwordx4 v75, s[12:15], s66 offen offset:2048 lds\n"
    "buffer_load_dwordx4 v75, s[12:15], s66 offen offset:3072 lds\n"
    "s_add_u32 m0, s85, 0x10000\n"
    "s_nop 0\n"
    "buffer_load_dwordx4 v72, s[8:11], s59 offen lds\n"
    "buffer_load_dwordx4 v73, s[8:11], s59 offen offset:1024 lds\n"
    "s_add_u32 m0, s85, 0x18000\n"
    "s_nop 0\n"
    "buffer_load_dwordx4 v74, s[12:15], s66 offen lds\n"
    "buffer_load_dwordx4 v75, s[12:15], s66 offen offset:1024 lds\n"
    "s_mov_b32 s28, s56\n"
    "s_and_b32 s29, s57, 0xffff\n"
    "s_mov_b32 s30, 0x1014000\n"
    "s_mov_b32 s31, 0x20000\n"
    "s_and_b32 s79, s75, 15\n"
    "s_lshl_b32 s79, s79, 20\n"
    "s_add_u32 s88, s79, s28\n"
    "s_lshr_b32 s88, s88, 17\n"
    "s_xor_b32 s88, s88, s75\n"
    "s_and_b32 s88, s88, 16\n"
    "s_lshl_b32 s88, s88, 9\n"
    "s_add_u32 s79, s79, s88\n"
    "v_lshrrev_b32 v32, 1, v34\n"
    "v_add_u32 v38, s75, v32\n"
    "v_and_b32 v38, 31, v38\n"
    "v_lshlrev_b32 v38, 2, v38\n"
    "s_mov_b32 s88, 0x1000000\n"
    "buffer_load_dword v26, v38, s[28:31], s88 offen sc1\n"
    "v_mov_b32 v39, 0x40000000\n"
    "s_lshl_b32 s88, s74, 14\n"
    "s_add_u32 s4, s79, 0xe8000\n"
    "s_add_u32 s4, s4, s88\n"
    "s_lshl_b32 s3, s76, 10\n"
    "s_add_u32 s3, s3, s4\n"
    "s_add_u32 s88, s28, s3\n"
    "s_addc_u32 s89, s29, 0\n"
    "s_add_u32 s90, s88, 0x2000\n"
    "s_addc_u32 s91, s89, 0\n"
    "v_writelane_b32 v28, s44, 0\n"
    "v_writelane_b32 v29, s45, 0\n"
    "v_writelane_b32 v28, s46, 1\n"
    "v_writelane_b32 v29, s47, 1\n"
    "v_writelane_b32 v28, s48, 2\n"
    "v_writelane_b32 v29, s49, 2\n"
    "v_writelane_b32 v28, s50, 3\n"
    "v_writelane_b32 v29, s51, 3\n"
    "v_writelane_b32 v28, s52, 4\n"
    "v_writelane_b32 v29, s53, 4\n"
    "v_writelane_b32 v28, s54, 5\n"
    "v_writelane_b32 v29, s55, 5\n"
    "v_writelane_b32 v28, s88, 6\n"
    "v_writelane_b32 v29, s89, 6\n"
    "v_writelane_b32 v28, s90, 7\n"
    "v_writelane_b32 v29, s91, 7\n"
    "s_mov_b64 exec, 0xff\n"
    "global_load_dword v16, v[28:29], off\n"
    "s_mov_b64 exec, -1\n"
    "s_add_u32 s88, s78, 2\n"
    "s_and_b32 s88, s88, 7\n"
    "s_lshl_b32 s60, s88, 7\n"
    "s_add_u32 s60, s60, s94\n"
    "s_mul_i32 s68, s88, 196608\n"
    "s_add_u32 s88, s78, 3\n"
    "s_and_b32 s88, s88, 7\n"
    "s_lshl_b32 s61, s88, 7\n"
    "s_add_u32 s61, s61, s94\n"
    "s_mul_i32 s69, s88, 196608\n"
    "s_add_u32 s88, s78, 4\n"
    "s_and_b32 s88, s88, 7\n"
    "s_lshl_b32 s62, s88, 7\n"
    "s_add_u32 s62, s62, s94\n"
    "s_mul_i32 s70, s88, 196608\n"
    "s_add_u32 s88, s78, 5\n"
    "s_and_b32 s88, s88, 7\n"
    "s_lshl_b32 s63, s88, 7\n"
    "s_add_u32 s63, s63, s94\n"
    "s_mul_i32 s71, s88, 196608\n"
    "s_add_u32 s88, s78, 6\n"
    "s_and_b32 s88, s88, 7\n"
    "s_lshl_b32 s64, s88, 7\n"
    "s_add_u32 s64, s64, s94\n"
    "s_mul_i32 s72, s88, 196608\n"
    "s_add_u32 s88, s78, 7\n"
    "s_and_b32 s88, s88, 7\n"
    "s_lshl_b32 s65, s88, 7\n"
    "s_add_u32 s65, s65, s94\n"
    "s_mul_i32 s73, s88, 196608\n"
    "s_mov_b32 s16, s44\n"
    "s_and_b32 s17, s45, 0xffff\n"
    "s_mov_b32 s18, 0x80000\n"
    "s_mov_b32 s19, 0x20000\n"
    "s_sub_u32 s46, s46, 2048\n"
    "s_subb_u32 s47, s47, 0\n"
    "s_mov_b32 s20, s46\n"
    "s_and_b32 s21, s47, 0xffff\n"
    "s_mov_b32 s22, 0x158800\n"
    "s_mov_b32 s23, 0x20000\n"
    "s_mov_b32 s24, s50\n"
    "s_and_b32 s25, s51, 0xffff\n"
    "s_mov_b32 s26, 0x158000\n"
    "s_mov_b32 s27, 0x20000\n"
    "s_mov_b32 s32, s54\n"
    "s_and_b32 s33, s55, 0xffff\n"
    "s_mov_b32 s34, 0x200000\n"
    "s_mov_b32 s35, 0x20000\n"
    "s_mov_b32 s36, s48\n"
    "s_and_b32 s37, s49, 0xffff\n"
    "s_mov_b32 s38, 0xac0\n"
    "s_mov_b32 s39, 0x20000\n"
    "s_mov_b64 s[6:7], s[52:53]\n"
    "s_lshl_b32 s88, s76, 7\n"
    "s_add_u32 s94, s94, s88\n"
    "v_subrev_u32 v38, 2, v34\n"
    "v_lshlrev_b32 v30, 2, v38\n"
    "v_cmp_lt_u32 vcc, 7, v38\n"
    "v_lshlrev_b32 v31, 2, v34\n"
    "v_lshl_add_u32 v31, v32, 25, v31\n"
    "v_cndmask_b32 v30, v30, v39, vcc\n"
    "v_cmp_le_u32 s[98:99], 8, v32\n"
    "v_cmp_eq_u32 vcc, s76, v32\n"
    "v_and_b32 v38, 1, v34\n"
    "v_lshlrev_b32 v33, 10, v32\n"
    "v_lshl_add_u32 v33, v38, 2, v33\n"
    "s_or_b64 s[98:99], vcc, s[98:99]\n"
    "v_cndmask_b32 v33, v33, v39, s[98:99]\n"
    "s_min_u32 s92, s75, 4\n"
    "v_cmp_le_u32 s[82:83], s92, v32\n"
    "s_sub_u32 s91, s3, s79\n"
    "s_lshl_b32 s88, s76, 3\n"
    "s_and_b32 s89, s74, 2\n"
    "s_add_u32 s88, s88, s89\n"
    "s_add_u32 s88, s88, s74\n"
    "s_lshl_b32 s90, s88, 12\n"
    "s_add_u32 s5, s90, s79\n"
    "s_sub_u32 s92, s75, 1\n"
    "s_and_b32 s41, s92, 15\n"
    "s_lshl_b32 s41, s41, 20\n"
    "s_add_u32 s88, s41, s28\n"
    "s_lshr_b32 s88, s88, 17\n"
    "s_xor_b32 s88, s88, s92\n"
    "s_and_b32 s88, s88, 16\n"
    "s_lshl_b32 s88, s88, 9\n"
    "s_add_u32 s41, s41, s88\n"
    "s_add_u32 s88, s41, s91\n"
    "v_writelane_b32 v32, s88, 0\n"
    "v_writelane_b32 v32, s88, 1\n"
    "s_add_u32 s41, s41, s90\n"
    "s_cmp_lt_u32 s75, 1\n"
    "s_cselect_b32 s41, 0x40000000, s41\n"
    "s_sub_u32 s92, s75, 2\n"
    "s_and_b32 s42, s92, 15\n"
    "s_lshl_b32 s42, s42, 20\n"
    "s_add_u32 s88, s42, s28\n"
    "s_lshr_b32 s88, s88, 17\n"
    "s_xor_b32 s88, s88, s92\n"
    "s_and_b32 s88, s88, 16\n"
    "s_lshl_b32 s88, s88, 9\n"
    "s_add_u32 s42, s42, s88\n"
    "s_add_u32 s88, s42, s91\n"
    "v_writelane_b32 v32, s88, 2\n"
    "v_writelane_b32 v32, s88, 3\n"
    "s_add_u32 s42, s42, s90\n"
    "s_cmp_lt_u32 s75, 2\n"
    "s_cselect_b32 s42, 0x40000000, s42\n"
    "s_sub_u32 s92, s75, 3\n"
    "s_and_b32 s43, s92, 15\n"
    "s_lshl_b32 s43, s43, 20\n"
    "s_add_u32 s88, s43, s28\n"
    "s_lshr_b32 s88, s88, 17\n"
    "s_xor_b32 s88, s88, s92\n"
    "s_and_b32 s88, s88, 16\n"
    "s_lshl_b32 s88, s88, 9\n"
    "s_add_u32 s43, s43, s88\n"
    "s_add_u32 s88, s43, s91\n"
    "v_writelane_b32 v32, s88, 4\n"
    "v_writelane_b32 v32, s88, 5\n"
    "s_add_u32 s43, s43, s90\n"
    "s_cmp_lt_u32 s75, 3\n"
    "s_cselect_b32 s43, 0x40000000, s43\n"
    "s_sub_u32 s92, s75, 4\n"
    "s_and_b32 s44, s92, 15\n"
    "s_lshl_b32 s44, s44, 20\n"
    "s_add_u32 s88, s44, s28\n"
    "s_lshr_b32 s88, s88, 17\n"
    "s_xor_b32 s88, s88, s92\n"
    "s_and_b32 s88, s88, 16\n"
    "s_lshl_b32 s88, s88, 9\n"
    "s_add_u32 s44, s44, s88\n"
    "s_add_u32 s88, s44, s91\n"
    "v_writelane_b32 v32, s88, 6\n"
    "v_writelane_b32 v32, s88, 7\n"
    "s_add_u32 s44, s44, s90\n"
    "s_cmp_lt_u32 s75, 4\n"
    "s_cselect_b32 s44, 0x40000000, s44\n"
    "v_lshl_add_u32 v32, v34, 2, v32\n"
    "v_cndmask_b32 v32, v32, v39, s[82:83]\n"
    "s_min_u32 s88, s76, 3\n"
    "s_mul_i32 s89, s76, 5\n"
    "s_add_u32 s93, s88, s89\n"
    "v_and_b32 v38, 2, v36\n"
    "v_lshrrev_b32 v39, 2, v36\n"
    "v_or_b32 v38, v38, v39\n"
    "v_lshlrev_b32 v38, 1, v38\n"
    "v_xor_b32 v38, v38, v37\n"
    "v_lshlrev_b32 v38, 4, v38\n"
    "s_lshl_b32 s88, s76, 7\n"
    "v_lshl_add_u32 v39, v36, 10, v38\n"
    "v_add_u32 v39, s88, v39\n"
    "s_lshl_b32 s89, s74, 14\n"
    "v_add_u32 v3, s89, v39\n"
    "v_add_u32 v4, 0x1c00, v3\n"
    "v_add_u32 v6, s89, v3\n"
    "v_add_u32 v7, 0x20000, v3\n"
    "s_mul_i32 s89, s74, 22016\n"
    "s_lshl_b32 s88, s93, 6\n"
    "s_add_u32 s89, s89, s88\n"
    "v_mul_u32_u24 v5, 2752, v36\n"
    "v_add3_u32 v5, v5, v38, s89\n"
    "s_cmp_ge_u32 s76, 3\n"
    "s_cselect_b32 s88, 64, 0\n"
    "v_and_b32 v38, s88, v38\n"
    "v_lshl_add_u32 v24, v38, 24, v5\n"
    "v_add_u32 v24, 256, v24\n"
    "v_add_u32 v17, 1152, v5\n"
    "v_add_u32 v5, 2048, v5\n"
    "v_lshlrev_b32 v1, 4, v34\n"
    "s_lshl_b32 s88, s74, 14\n"
    "v_add_u32 v2, s88, v1\n"
    "s_lshl_b32 s88, %1, 2\n"
    "s_add_u32 s88, s88, s74\n"
    "s_lshr_b32 s89, %1, 3\n"
    "s_lshl_b32 s89, s89, 2\n"
    "s_add_u32 s89, s89, s74\n"
    "v_mov_b32 v39, 0x40000000\n"
    "v_and_b32 v36, 15, v34\n"
    "v_lshrrev_b32 v37, 4, v34\n"
    "s_mul_i32 s90, s88, 512\n"
    "v_lshl_add_u32 v88, v36, 7, s90\n"
    "s_lshr_b32 s91, s16, 7\n"
    "s_sub_u32 s91, 3, s91\n"
    "s_and_b32 s91, s91, 7\n"
    "s_xor_b32 s92, s91, s76\n"
    "s_lshl_b32 s91, s91, 7\n"
    "s_mul_i32 s90, s89, 4096\n"
    "s_add_u32 s90, s90, s91\n"
    "s_cmp_lt_u32 s92, 4\n"
    "s_cselect_b32 s90, s90, 0x40000000\n"
    "v_lshl_add_u32 v38, v36, 10, s90\n"
    "v_cmp_eq_u32 vcc, 1, v37\n"
    "s_nop 1\n"
    "v_cndmask_b32 v88, v88, v38, vcc\n"
    "v_cmp_gt_u32 s[90:91], 2, v37\n"
    "v_cmp_gt_u32 vcc, 4, v36\n"
    "s_nop 0\n"
    "s_and_b64 vcc, vcc, s[90:91]\n"
    "v_cndmask_b32 v88, v39, v88, vcc\n"
    "s_mul_i32 s90, s88, 1408\n"
    "v_lshl_add_u32 v89, v36, 7, s90\n"
    "v_cmp_gt_u32 s[90:91], 1, v37\n"
    "v_cmp_gt_u32 vcc, 11, v36\n"
    "s_nop 0\n"
    "s_and_b64 vcc, vcc, s[90:91]\n"
    "v_cndmask_b32 v89, v39, v89, vcc\n"
    "s_mul_i32 s90, s88, 1408\n"
    "v_lshl_add_u32 v90, v36, 7, s90\n"
    "s_lshr_b32 s91, s24, 7\n"
    "s_sub_u32 s91, 3, s91\n"
    "s_and_b32 s91, s91, 7\n"
    "s_xor_b32 s92, s91, s76\n"
    "s_lshl_b32 s91, s91, 7\n"
    "s_mul_i32 s90, s89, 11264\n"
    "s_add_u32 s90, s90, s91\n"
    "s_cmp_lt_u32 s92, 4\n"
    "s_cselect_b32 s90, s90, 0x40000000\n"
    "v_lshl_add_u32 v38, v36, 10, s90\n"
    "v_cmp_eq_u32 vcc, 1, v37\n"
    "s_nop 1\n"
    "v_cndmask_b32 v90, v90, v38, vcc\n"
    "v_cmp_gt_u32 s[90:91], 2, v37\n"
    "v_cmp_gt_u32 vcc, 11, v36\n"
    "s_nop 0\n"
    "s_and_b64 vcc, vcc, s[90:91]\n"
    "v_cndmask_b32 v90, v39, v90, vcc\n"
    "v_and_b32 v36, 15, v34\n"
    "v_lshrrev_b32 v38, 1, v36\n"
    "s_lshl_b32 s88, s74, 1\n"
    "v_lshrrev_b32 v84, 5, v34\n"
    "v_add_u32 v84, s88, v84\n"
    "v_xor_b32 v84, v84, v38\n"
    "v_lshlrev_b32 v84, 4, v84\n"
    "v_lshl_add_u32 v84, v36, 7, v84\n"
    "v_bfe_u32 v38, v34, 4, 1\n"
    "v_lshl_add_u32 v84, v38, 3, v84\n"
    "v_add_u32 v84, 0x12000, v84\n"
    "v_and_b32 v36, 15, v34\n"
    "v_lshrrev_b32 v37, 4, v34\n"
    "v_lshrrev_b32 v38, 1, v36\n"
    "v_xor_b32 v78, v38, v37\n"
    "v_lshlrev_b32 v78, 4, v78\n"
    "v_lshl_add_u32 v78, v36, 7, v78\n"
    "v_add_u32 v78, 0x10000, v78\n"
    "v_xor_b32 v79, 64, v78\n"
    "v_add_u32 v80, s85, v78\n"
    "v_add_u32 v81, s85, v79\n"
    "s_lshl_b32 s88, s74, 1\n"
    "v_and_b32 v38, 1, v37\n"
    "v_lshrrev_b32 v39, 2, v36\n"
    "v_and_b32 v82, 2, v39\n"
    "v_or_b32 v82, v82, v38\n"
    "v_lshlrev_b32 v82, 1, v82\n"
    "v_bfe_u32 v83, v36, 1, 1\n"
    "v_add_u32 v83, s88, v83\n"
    "v_xor_b32 v82, v82, v83\n"
    "v_lshlrev_b32 v82, 4, v82\n"
    "v_lshl_add_u32 v82, v39, 7, v82\n"
    "v_and_b32 v83, 1, v36\n"
    "v_lshl_add_u32 v82, v83, 3, v82\n"
    "v_lshl_add_u32 v25, v38, 9, v82\n"
    "v_lshrrev_b32 v83, 1, v37\n"
    "v_lshl_add_u32 v25, v83, 11, v25\n"
    "v_add_u32 v25, 0x18000, v25\n"
    "v_lshl_add_u32 v83, v37, 10, v82\n"
    "v_add_u32 v83, 0x18000, v83\n"
    "v_lshl_add_u32 v82, v37, 11, v82\n"
    "s_lshl_b32 s88, s74, 6\n"
    "s_add_u32 s88, s88, 0x10000\n"
    "v_lshl_add_u32 v85, v36, 2, s88\n"
    "v_cmp_eq_u32 vcc, v39, v37\n"
    "v_mov_b32 v38, 0x10400\n"
    "v_lshlrev_b32 v86, 2, v36\n"
    "v_cndmask_b32 v85, v38, v85, vcc\n"
    "v_add_u32 v86, 0x10000, v86\n"
    "v_lshlrev_b32 v87, 4, v37\n"
    "v_add_u32 v87, 0x10000, v87\n"
    "v_bfe_u32 v36, v34, 2, 2\n"
    "v_bfe_u32 v37, v34, 4, 1\n"
    "v_lshrrev_b32 v38, 5, v34\n"
    "v_and_b32 v39, 2, v36\n"
    "v_xor_b32 v39, v39, v37\n"
    "v_xor_b32 v39, v39, v38\n"
    "v_and_b32 v18, 3, v34\n"
    "v_lshlrev_b32 v18, 3, v18\n"
    "v_lshl_add_u32 v18, v36, 7, v18\n"
    "v_lshl_add_u32 v18, v39, 5, v18\n"
    "v_lshl_add_u32 v18, v38, 9, v18\n"
    "v_mov_b32 v52, 0x3f803f80\n"
    "v_mov_b32 v53, 0x3f803f80\n"
    "v_mov_b32 v54, 0x3f803f80\n"
    "v_mov_b32 v55, 0x3f803f80\n"
    "v_mov_b32 v60, 0x3f803f80\n"
    "v_mov_b32 v61, 0x3f803f80\n"
    "v_mov_b32 v62, 0x3f803f80\n"
    "v_mov_b32 v63, 0x3f803f80\n"
    "s_cmp_ge_u32 s75, 4\n"
    "s_cselect_b32 s91, 0x3f803f80, 0\n"
    "v_mov_b32 v108, s91\n"
    "v_mov_b32 v109, s91\n"
    "v_mov_b32 v110, s91\n"
    "v_mov_b32 v111, s91\n"
    "v_mov_b32 v116, s91\n"
    "v_mov_b32 v117, s91\n"
    "v_mov_b32 v118, s91\n"
    "v_mov_b32 v119, s91\n"
    "s_cmp_ge_u32 s75, 3\n"
    "s_cselect_b32 s91, 0x3f803f80, 0\n"
    "v_mov_b32 v124, s91\n"
    "v_mov_b32 v125, s91\n"
    "v_mov_b32 v126, s91\n"
    "v_mov_b32 v127, s91\n"
    "v_mov_b32 v132, s91\n"
    "v_mov_b32 v133, s91\n"
    "v_mov_b32 v134, s91\n"
    "v_mov_b32 v135, s91\n"
    "s_cmp_ge_u32 s75, 2\n"
    "s_cselect_b32 s91, 0x3f803f80, 0\n"
    "v_mov_b32 v140, s91\n"
    "v_mov_b32 v141, s91\n"
    "v_mov_b32 v142, s91\n"
    "v_mov_b32 v143, s91\n"
    "v_mov_b32 v148, s91\n"
    "v_mov_b32 v149, s91\n"
    "v_mov_b32 v150, s91\n"
    "v_mov_b32 v151, s91\n"
    "s_cmp_ge_u32 s75, 1\n"
    "s_cselect_b32 s91, 0x3f803f80, 0\n"
    "v_mov_b32 v156, s91\n"
    "v_mov_b32 v157, s91\n"
    "v_mov_b32 v158, s91\n"
    "v_mov_b32 v159, s91\n"
    "v_mov_b32 v164, s91\n"
    "v_mov_b32 v165, s91\n"
    "v_mov_b32 v166, s91\n"
    "v_mov_b32 v167, s91\n"
    "v_and_b32 v36, 31, v34\n"
    "v_lshrrev_b32 v37, 5, v34\n"
    "v_lshlrev_b32 v37, 2, v37\n"
    "v_sub_u32 v36, v36, v37\n"
    "v_mov_b32 v37, 0xf149f2ca\n"
    "s_mul_i32 s78, s74, 49152\n"
    "s_mul_i32 s45, s74, 3072\n"
    "s_mul_i32 s46, s74, 6144\n"
    "s_lshl_b32 s47, s74, 13\n"
    "v_min_u32 v91, 5, v34\n"
    "v_lshlrev_b32 v91, 7, v91\n"
    "v_and_b32 v29, 1, v34\n"
    "s_lshl_b32 s88, s75, 3\n"
    "s_add_u32 s88, s88, 0x1011000\n"
    "v_bfe_u32 v39, v34, 1, 3\n"
    "v_lshl_add_u32 v39, v39, 8, s88\n"
    "v_lshl_add_u32 v39, v29, 2, v39\n"
    "v_mul_u32_u24 v38, 12, v29\n"
    "s_mov_b32 s48, 0x22222222\n"
    "s_mov_b32 s49, 0x22222222\n"
    "s_mov_b32 s50, 0x44444444\n"
    "s_mov_b32 s51, 0x44444444\n"
    "s_mov_b32 s52, 0x88888888\n"
    "s_mov_b32 s53, 0x88888888\n"
    "s_mov_b32 s54, 0xaaaaaaaa\n"
    "s_mov_b32 s55, 0xaaaaaaaa\n"
    "s_add_u32 m0, s84, 0x4000\n"
    "s_nop 0\n"
    "buffer_load_dwordx4 v76, s[12:15], s67 offen lds\n"
    "buffer_load_dwordx4 v76, s[12:15], s67 offen offset:1024 lds\n"
    "buffer_load_dwordx4 v75, s[12:15], s67 offen offset:2048 lds\n"
    "buffer_load_dwordx4 v75, s[12:15], s67 offen offset:3072 lds\n"
    "s_add_u32 m0, s85, 0x12000\n"
    "s_nop 0\n"
    "buffer_load_dwordx4 v72, s[8:11], s60 offen lds\n"
    "buffer_load_dwordx4 v73, s[8:11], s60 offen offset:1024 lds\n"
    "s_add_u32 m0, s84, 0x8000\n"
    "s_nop 0\n"
    "buffer_load_dwordx4 v76, s[12:15], s68 offen lds\n"
    "s_waitcnt vmcnt(13)\n"
    "s_barrier\n"
    "ds_read_b128 v[232:235], v80 offset:40960\n"
    "ds_read_b128 v[92:95], v81 offset:40960\n"
    "s_mov_b32 s91, 0x9E3779B5\n"
    "s_add_u32 s66, s79, 0x60000\n"
    "ds_read_b64_tr_b16 v[216:217], v82\n"
    "ds_read_b64_tr_b16 v[218:219], v82 offset:512\n"
    "ds_read_b128 a[0:3], v78 offset:40960\n"
    "ds_read_b128 a[4:7], v78 offset:43008\n"
    "ds_read_b128 a[8:11], v78 offset:45056\n"
    "ds_read_b128 a[12:15], v78 offset:47104\n"
    "ds_read_b64_tr_b16 v[224:225], v82 offset:1024\n"
    "s_waitcnt lgkmcnt(8)\n"
    "v_mfma_f32_16x16x32_bf16 v[208:211], v[60:63], v[232:235], 0\n"
    "ds_read_b64_tr_b16 v[226:227], v82 offset:1536\n"
    "ds_read_b64_tr_b16 v[220:221], v82 offset:8192\n"
    "ds_read_b64_tr_b16 v[222:223], v82 offset:8704\n"
    "v_mfma_f32_16x16x32_bf16 v[212:215], v[232:235], v[232:235], 0\n"
    "ds_read_b64_tr_b16 v[228:229], v82 offset:9216\n"
    "ds_read_b64_tr_b16 v[230:231], v82 offset:9728\n"
    "s_waitcnt lgkmcnt(11)\n"
    "v_mfma_f32_16x16x32_bf16 v[208:211], v[60:63], v[92:95], v[208:211]\n"
    "ds_read_b128 a[16:19], v79 offset:40960\n"
    "ds_read_b128 a[20:23], v79 offset:43008\n"
    "v_mfma_f32_16x16x32_bf16 v[212:215], v[92:95], v[92:95], v[212:215]\n"
    "ds_read_b128 a[24:27], v79 offset:45056\n"
    "s_waitcnt lgkmcnt(11)\n"
    "v_mfma_f32_16x16x32_bf16 v[184:187], v[216:219], v[60:63], 0\n"
    "ds_read_b128 a[28:31], v79 offset:47104\n"
    "v_mfma_f32_16x16x32_bf16 v[168:171], v[216:219], a[0:3], 0\n"
    "v_mfma_f32_16x16x32_bf16 v[172:175], v[216:219], a[4:7], 0\n"
    "s_waitcnt lgkmcnt(8)\n"
    "v_mfma_f32_16x16x32_bf16 v[176:179], v[216:219], a[8:11], 0\n"
    "buffer_load_dwordx4 v76, s[12:15], s68 offen offset:1024 lds\n"
    "buffer_load_dwordx4 v75, s[12:15], s68 offen offset:2048 lds\n"
    "v_mfma_f32_16x16x32_bf16 v[180:183], v[216:219], a[12:15], 0\n"
    "s_waitcnt vmcnt(13)\n"
    "s_barrier\n"
    "buffer_load_dwordx4 v75, s[12:15], s68 offen offset:3072 lds\n"
    "ds_read_b128 v[232:235], v80\n"
    "ds_read_b128 v[92:95], v81\n"
    "v_mfma_f32_16x16x32_bf16 v[204:207], v[60:63], v[224:227], 0\n"
    "ds_read_b128 a[32:35], v78\n"
    "s_waitcnt vmcnt(11)\n"
    "v_mul_lo_u32 v26, v26, s91\n"
    "v_mfma_f32_16x16x32_bf16 v[188:191], a[0:3], v[224:227], 0\n"
    "s_add_u32 m0, s85, 0x14000\n"
    "ds_read_b128 a[36:39], v78 offset:2048\n"
    "buffer_load_dwordx4 v72, s[8:11], s61 offen lds\n"
    "ds_read_b128 a[40:43], v78 offset:4096\n"
    "v_mfma_f32_16x16x32_bf16 v[192:195], a[4:7], v[224:227], 0\n"
    "ds_read_b128 a[44:47], v78 offset:6144\n"
    "v_add_u32 v26, 0x7F4A7C15, v26\n"
    "v_mfma_f32_16x16x32_bf16 v[196:199], a[8:11], v[224:227], 0\n"
    "ds_read_b128 a[48:51], v79\n"
    "s_mov_b32 s91, 0x85EBCA6B\n"
    "v_mfma_f32_16x16x32_bf16 v[200:203], a[12:15], v[224:227], 0\n"
    "buffer_load_dwordx4 v73, s[8:11], s61 offen offset:1024 lds\n"
    "ds_read_b128 a[52:55], v79 offset:2048\n"
    "ds_read_b128 a[56:59], v79 offset:4096\n"
    "s_waitcnt lgkmcnt(8)\n"
    "v_mfma_f32_16x16x32_bf16 v[184:187], v[220:223], v[60:63], v[184:187]\n"
    "ds_read_b128 a[60:63], v79 offset:6144\n"
    "v_mul_lo_u32 v27, v26, s91\n"
    "v_mfma_f32_16x16x32_bf16 v[168:171], v[220:223], a[16:19], v[168:171]\n"
    "v_readfirstlane_b32 s80, v26\n"
    "v_xor_b32 v27, 0x5BD1E995, v27\n"
    "s_add_u32 m0, s84, 0xc000\n"
    "v_mfma_f32_16x16x32_bf16 v[172:175], v[220:223], a[20:23], v[172:175]\n"
    "buffer_load_dwordx4 v76, s[12:15], s69 offen lds\n"
    "v_mfma_f32_16x16x32_bf16 v[176:179], v[220:223], a[24:27], v[176:179]\n"
    "v_readfirstlane_b32 s81, v27\n"
    "v_cndmask_b32 v26, v26, v27, s[54:55]\n"
    "v_mfma_f32_16x16x32_bf16 v[180:183], v[220:223], a[28:31], v[180:183]\n"
    "v_mov_b32 v27, s81\n"
    "v_mov_b32 v28, s80\n"
    "v_mfma_f32_16x16x32_bf16 v[204:207], v[60:63], v[228:231], v[204:207]\n"
    "v_cndmask_b32 v27, v28, v27, s[54:55]\n"
    "buffer_load_dwordx4 v76, s[12:15], s69 offen offset:1024 lds\n"
    "v_mfma_f32_16x16x32_bf16 v[188:191], a[16:19], v[228:231], v[188:191]\n"
    "v_mfma_f32_16x16x32_bf16 v[192:195], a[20:23], v[228:231], v[192:195]\n"
    "v_mfma_f32_16x16x32_bf16 v[196:199], a[24:27], v[228:231], v[196:199]\n"
    "buffer_load_dwordx4 v75, s[12:15], s69 offen offset:2048 lds\n"
    "v_mfma_f32_16x16x32_bf16 v[200:203], a[28:31], v[228:231], v[200:203]\n"
    "s_waitcnt vmcnt(9)\n"
    "s_barrier\n"
    "buffer_load_dwordx4 v75, s[12:15], s69 offen offset:3072 lds\n"
    "ds_read_b64_tr_b16 v[216:217], v82 offset:16384\n"
    "ds_read_b64_tr_b16 v[218:219], v82 offset:16896\n"
    "ds_read_b64_tr_b16 v[224:225], v82 offset:17408\n"
    "ds_read_b64_tr_b16 v[226:227], v82 offset:17920\n"
    "ds_read_b64_tr_b16 v[220:221], v82 offset:24576\n"
    "v_mfma_f32_16x16x32_bf16 v[208:211], v[60:63], v[232:235], v[208:211]\n"
    "ds_read_b64_tr_b16 v[222:223], v82 offset:25088\n"
    "ds_read_b64_tr_b16 v[228:229], v82 offset:25600\n"
    "v_mfma_f32_16x16x32_bf16 v[212:215], v[232:235], v[232:235], v[212:215]\n"
    "s_add_u32 m0, s85, 0x16000\n"
    "ds_read_b64_tr_b16 v[230:231], v82 offset:26112\n"
    "buffer_load_dwordx4 v72, s[8:11], s62 offen lds\n"
    "ds_read_b128 v[232:235], v80 offset:8192\n"
    "ds_read_b128 a[64:67], v78 offset:8192\n"
    "s_waitcnt lgkmcnt(10)\n"
    "v_mfma_f32_16x16x32_bf16 v[208:211], v[60:63], v[92:95], v[208:211]\n"
    "ds_read_b128 a[68:71], v78 offset:10240\n"
    "s_and_b32 s88, %1, 0xe6\n"
    "s_or_b32 s88, s88, s74\n"
    "s_cbranch_scc0 L_pb_%=\n"
    "L_pbd_%=:\n"
    "ds_read_b128 a[72:75], v78 offset:12288\n"
    "buffer_load_dwordx4 v73, s[8:11], s62 offen offset:1024 lds\n"
    "ds_read_b128 a[76:79], v78 offset:14336\n"
    "v_mfma_f32_16x16x32_bf16 v[212:215], v[92:95], v[92:95], v[212:215]\n"
    "ds_read_b128 v[92:95], v81 offset:8192\n"
    "v_cmp_le_i32 s[90:91], 1, v36\n"
    "s_add_u32 s62, s79, 0x40000\n"
    "ds_read_b128 a[80:83], v79 offset:8192\n"
    "ds_read_b128 a[84:87], v79 offset:10240\n"
    "s_waitcnt lgkmcnt(7)\n"
    "s_add_u32 m0, s85, 0x1c000\n"
    "v_mfma_f32_16x16x32_bf16 v[208:211], v[60:63], v[232:235], v[208:211]\n"
    "buffer_load_dwordx4 v74, s[12:15], s67 offen lds\n"
    "ds_read_b128 a[88:91], v79 offset:12288\n"
    "ds_read_b128 a[92:95], v79 offset:14336\n"
    "v_mfma_f32_16x16x32_bf16 v[212:215], v[232:235], v[232:235], v[212:215]\n"
    "ds_read_b64_tr_b16 v[252:253], v83\n"
    "ds_read_b64_tr_b16 v[254:255], v83 offset:512\n"
    "ds_read_b64_tr_b16 v[236:237], v83 offset:4096\n"
    "buffer_load_dwordx4 v75, s[12:15], s67 offen offset:1024 lds\n"
    "ds_read_b64_tr_b16 v[238:239], v83 offset:4608\n"
    "s_waitcnt lgkmcnt(8)\n"
    "v_mfma_f32_16x16x32_bf16 v[208:211], v[60:63], v[92:95], v[208:211]\n"
    "s_add_u32 s67, s79, 0x61000\n"
    "v_mfma_f32_16x16x32_bf16 v[212:215], v[92:95], v[92:95], v[212:215]\n"
    "s_add_u32 m0, s84, 0x0\n"
    "v_mfma_f32_16x16x32_bf16 v[184:187], v[216:219], v[60:63], v[184:187]\n"
    "buffer_load_dwordx4 v76, s[12:15], s70 offen lds\n"
    "v_mfma_f32_16x16x32_bf16 v[168:171], v[216:219], a[32:35], v[168:171]\n"
    "v_mfma_f32_16x16x32_bf16 v[172:175], v[216:219], a[36:39], v[172:175]\n"
    "v_mfma_f32_16x16x32_bf16 v[176:179], v[216:219], a[40:43], v[176:179]\n"
    "buffer_load_dwordx4 v76, s[12:15], s70 offen offset:1024 lds\n"
    "s_waitcnt vmcnt(10)\n"
    "s_barrier\n"
    "ds_read_b128 v[232:235], v80 offset:16384\n"
    "ds_read_b128 v[92:95], v81 offset:16384\n"
    "buffer_load_dwordx4 v75, s[12:15], s70 offen offset:2048 lds\n"
    "ds_read_b128 a[96:99], v78 offset:16384\n"
    "v_mfma_f32_16x16x32_bf16 v[180:183], v[216:219], a[44:47], v[180:183]\n"
    "ds_read_b64_tr_b16 v[216:217], v82 offset:32768\n"
    "ds_read_b64_tr_b16 v[218:219], v82 offset:33280\n"
    "v_mfma_f32_16x16x32_bf16 v[204:207], v[60:63], v[224:227], v[204:207]\n"
    "ds_read_b128 a[100:103], v78 offset:18432\n"
    "v_mfma_f32_16x16x32_bf16 v[188:191], a[32:35], v[224:227], v[188:191]\n"
    "ds_read_b128 a[104:107], v78 offset:20480\n"
    "buffer_load_dwordx4 v75, s[12:15], s70 offen offset:3072 lds\n"
    "ds_read_b128 a[108:111], v78 offset:22528\n"
    "v_mfma_f32_16x16x32_bf16 v[192:195], a[36:39], v[224:227], v[192:195]\n"
    "ds_read_b128 a[112:115], v79 offset:16384\n"
    "v_mfma_f32_16x16x32_bf16 v[196:199], a[40:43], v[224:227], v[196:199]\n"
    "ds_read_b128 a[116:119], v79 offset:18432\n"
    "v_mfma_f32_16x16x32_bf16 v[200:203], a[44:47], v[224:227], v[200:203]\n"
    "s_add_u32 m0, s85, 0x10000\n"
    "ds_read_b64_tr_b16 v[224:225], v82 offset:33792\n"
    "buffer_load_dwordx4 v72, s[8:11], s63 offen lds\n"
    "ds_read_b64_tr_b16 v[226:227], v82 offset:34304\n"
    "ds_read_b128 a[120:123], v79 offset:20480\n"
    "ds_read_b128 a[124:127], v79 offset:22528\n"
    "v_mfma_f32_16x16x32_bf16 v[184:187], v[220:223], v[60:63], v[184:187]\n"
    "v_mfma_f32_16x16x32_bf16 v[168:171], v[220:223], a[48:51], v[168:171]\n"
    "v_mfma_f32_16x16x32_bf16 v[172:175], v[220:223], a[52:55], v[172:175]\n"
    "buffer_load_dwordx4 v73, s[8:11], s63 offen offset:1024 lds\n"
    "v_mfma_f32_16x16x32_bf16 v[176:179], v[220:223], a[56:59], v[176:179]\n"
    "s_add_u32 s63, s79, 0x41000\n"
    "v_mfma_f32_16x16x32_bf16 v[180:183], v[220:223], a[60:63], v[180:183]\n"
    "ds_read_b64_tr_b16 v[220:221], v82 offset:40960\n"
    "ds_read_b64_tr_b16 v[222:223], v82 offset:41472\n"
    "s_add_u32 m0, s85, 0x1e000\n"
    "v_mfma_f32_16x16x32_bf16 v[204:207], v[60:63], v[228:231], v[204:207]\n"
    "buffer_load_dwordx4 v74, s[12:15], s68 offen lds\n"
    "v_mfma_f32_16x16x32_bf16 v[188:191], a[48:51], v[228:231], v[188:191]\n"
    "v_mfma_f32_16x16x32_bf16 v[192:195], a[52:55], v[228:231], v[192:195]\n"
    "v_mfma_f32_16x16x32_bf16 v[196:199], a[56:59], v[228:231], v[196:199]\n"
    "buffer_load_dwordx4 v75, s[12:15], s68 offen offset:1024 lds\n"
    "v_mfma_f32_16x16x32_bf16 v[200:203], a[60:63], v[228:231], v[200:203]\n"
    "ds_read_b64_tr_b16 v[228:229], v82 offset:41984\n"
    "ds_read_b64_tr_b16 v[230:231], v82 offset:42496\n"
    "s_waitcnt lgkmcnt(3)\n"
    "v_mfma_f32_16x16x32_bf16 v[208:211], v[60:63], v[232:235], v[208:211]\n"
    "s_add_u32 s68, s79, 0x70000\n"
    "s_add_u32 m0, s84, 0x4000\n"
    "v_mfma_f32_16x16x32_bf16 v[212:215], v[232:235], v[232:235], v[212:215]\n"
    "buffer_load_dwordx4 v76, s[12:15], s71 offen lds\n"
    "v_mfma_f32_16x16x32_bf16 v[208:211], v[60:63], v[92:95], v[208:211]\n"
    "v_mfma_f32_16x16x32_bf16 v[212:215], v[92:95], v[92:95], v[212:215]\n"
    "v_mfma_f32_16x16x32_bf16 v[184:187], v[216:219], v[60:63], v[184:187]\n"
    "v_mfma_f32_16x16x32_bf16 v[168:171], v[216:219], a[64:67], v[168:171]\n"
    "v_mfma_f32_16x16x32_bf16 v[172:175], v[216:219], a[68:71], v[172:175]\n"
    "buffer_load_dwordx4 v76, s[12:15], s71 offen offset:1024 lds\n"
    "v_mfma_f32_16x16x32_bf16 v[176:179], v[216:219], a[72:75], v[176:179]\n"
    "s_waitcnt lgkmcnt(0)\n"
    "s_waitcnt vmcnt(12)\n"
    "s_barrier\n"
    "buffer_load_dwordx4 v75, s[12:15], s71 offen offset:2048 lds\n"
    "v_mfma_f32_16x16x32_bf16 v[180:183], v[216:219], a[76:79], v[180:183]\n"
    "ds_read_b128 v[232:235], v80 offset:24576\n"
    "ds_read_b128 v[92:95], v81 offset:24576\n"
    "v_mfma_f32_16x16x32_bf16 v[204:207], v[60:63], v[224:227], v[204:207]\n"
    "ds_read_b64_tr_b16 v[216:217], v82 offset:49152\n"
    "v_mfma_f32_16x16x32_bf16 v[188:191], a[64:67], v[224:227], v[188:191]\n"
    "ds_read_b64_tr_b16 v[218:219], v82 offset:49664\n"
    "ds_read_b128 a[128:131], v78 offset:24576\n"
    "v_mfma_f32_16x16x32_bf16 v[192:195], a[68:71], v[224:227], v[192:195]\n"
    "ds_read_b128 a[132:135], v78 offset:26624\n"
    "v_mfma_f32_16x16x32_bf16 v[196:199], a[72:75], v[224:227], v[196:199]\n"
    "ds_read_b128 a[136:139], v78 offset:28672\n"
    "v_mfma_f32_16x16x32_bf16 v[200:203], a[76:79], v[224:227], v[200:203]\n"
    "ds_read_b64_tr_b16 v[224:225], v82 offset:50176\n"
    "v_mfma_f32_16x16x32_bf16 v[184:187], v[220:223], v[60:63], v[184:187]\n"
    "ds_read_b64_tr_b16 v[226:227], v82 offset:50688\n"
    "ds_read_b128 a[140:143], v78 offset:30720\n"
    "v_mfma_f32_16x16x32_bf16 v[168:171], v[220:223], a[80:83], v[168:171]\n"
    "buffer_load_dwordx4 v75, s[12:15], s71 offen offset:3072 lds\n"
    "s_add_u32 m0, s85, 0x12000\n"
    "s_nop 0\n"
    "buffer_load_dwordx4 v72, s[8:11], s64 offen lds\n"
    "ds_read_b128 a[144:147], v79 offset:24576\n"
    "ds_read_b128 a[148:151], v79 offset:26624\n"
    "v_mfma_f32_16x16x32_bf16 v[172:175], v[220:223], a[84:87], v[172:175]\n"
    "ds_read_b128 a[152:155], v79 offset:28672\n"
    "v_mfma_f32_16x16x32_bf16 v[176:179], v[220:223], a[88:91], v[176:179]\n"
    "ds_read_b128 a[156:159], v79 offset:30720\n"
    "buffer_load_dwordx4 v73, s[8:11], s64 offen offset:1024 lds\n"
    "v_mfma_f32_16x16x32_bf16 v[180:183], v[220:223], a[92:95], v[180:183]\n"
    "ds_read_b64_tr_b16 v[220:221], v82 offset:57344\n"
    "ds_read_b64_tr_b16 v[222:223], v82 offset:57856\n"
    "v_mfma_f32_16x16x32_bf16 v[204:207], v[60:63], v[228:231], v[204:207]\n"
    "s_add_u32 s64, s79, 0x50000\n"
    "v_mfma_f32_16x16x32_bf16 v[188:191], a[80:83], v[228:231], v[188:191]\n"
    "v_mfma_f32_16x16x32_bf16 v[192:195], a[84:87], v[228:231], v[192:195]\n"
    "v_mfma_f32_16x16x32_bf16 v[196:199], a[88:91], v[228:231], v[196:199]\n"
    "v_mfma_f32_16x16x32_bf16 v[200:203], a[92:95], v[228:231], v[200:203]\n"
    "ds_read_b64_tr_b16 v[228:229], v82 offset:58368\n"
    "ds_read_b64_tr_b16 v[230:231], v82 offset:58880\n"
    "s_waitcnt lgkmcnt(6)\n"
    "s_add_u32 m0, s85, 0x20000\n"
    "v_mfma_f32_16x16x32_bf16 v[208:211], v[60:63], v[232:235], v[208:211]\n"
    "buffer_load_dwordx4 v74, s[12:15], s69 offen lds\n"
    "v_mfma_f32_16x16x32_bf16 v[212:215], v[232:235], v[232:235], v[212:215]\n"
    "buffer_load_dwordx4 v75, s[12:15], s69 offen offset:1024 lds\n"
    "v_mfma_f32_16x16x32_bf16 v[208:211], v[60:63], v[92:95], v[208:211]\n"
    "s_add_u32 s69, s79, 0x71000\n"
    "v_mfma_f32_16x16x32_bf16 v[212:215], v[92:95], v[92:95], v[212:215]\n"
    "v_mfma_f32_16x16x32_bf16 v[184:187], v[216:219], v[60:63], v[184:187]\n"
    "v_mfma_f32_16x16x32_bf16 v[168:171], v[216:219], a[96:99], v[168:171]\n"
    "v_mfma_f32_16x16x32_bf16 v[172:175], v[216:219], a[100:103], v[172:175]\n"
    "v_mfma_f32_16x16x32_bf16 v[176:179], v[216:219], a[104:107], v[176:179]\n"
    "v_mfma_f32_16x16x32_bf16 v[180:183], v[216:219], a[108:111], v[180:183]\n"
    "v_mfma_f32_16x16x32_bf16 v[204:207], v[60:63], v[224:227], v[204:207]\n"
    "v_mfma_f32_16x16x32_bf16 v[188:191], a[96:99], v[224:227], v[188:191]\n"
    "v_mfma_f32_16x16x32_bf16 v[192:195], a[100:103], v[224:227], v[192:195]\n"
    "s_add_u32 m0, s84, 0x8000\n"
    "v_mfma_f32_16x16x32_bf16 v[196:199], a[104:107], v[224:227], v[196:199]\n"
    "buffer_load_dwordx4 v76, s[12:15], s72 offen lds\n"
    "v_mfma_f32_16x16x32_bf16 v[200:203], a[108:111], v[224:227], v[200:203]\n"
    "buffer_load_dwordx4 v76, s[12:15], s72 offen offset:1024 lds\n"
    "s_waitcnt lgkmcnt(0)\n"
    "v_mfma_f32_16x16x32_bf16 v[184:187], v[220:223], v[60:63], v[184:187]\n"
    "s_waitcnt vmcnt(12)\n"
    "s_barrier\n"
    "ds_read_b128 v[232:235], v80\n"
    "v_mfma_f32_16x16x32_bf16 v[168:171], v[220:223], a[112:115], v[168:171]\n"
    "ds_read_b128 v[92:95], v81\n"
    "ds_read_b64_tr_b16 v[216:217], v82\n"
    "v_mfma_f32_16x16x32_bf16 v[172:175], v[220:223], a[116:119], v[172:175]\n"
    "ds_read_b64_tr_b16 v[218:219], v82 offset:512\n"
    "ds_read_b64_tr_b16 v[224:225], v82 offset:1024\n"
    "v_mfma_f32_16x16x32_bf16 v[176:179], v[220:223], a[120:123], v[176:179]\n"
    "ds_read_b64_tr_b16 v[226:227], v82 offset:1536\n"
    "ds_read_b128 a[160:163], v78\n"
    "v_mfma_f32_16x16x32_bf16 v[180:183], v[220:223], a[124:127], v[180:183]\n"
    "ds_read_b64_tr_b16 v[220:221], v82 offset:8192\n"
    "v_mfma_f32_16x16x32_bf16 v[204:207], v[60:63], v[228:231], v[204:207]\n"
    "ds_read_b64_tr_b16 v[222:223], v82 offset:8704\n"
    "ds_read_b128 a[164:167], v78 offset:2048\n"
    "v_mfma_f32_16x16x32_bf16 v[188:191], a[112:115], v[228:231], v[188:191]\n"
    "ds_read_b128 a[168:171], v78 offset:4096\n"
    "v_mfma_f32_16x16x32_bf16 v[192:195], a[116:119], v[228:231], v[192:195]\n"
    "ds_read_b128 a[172:175], v78 offset:6144\n"
    "buffer_load_dwordx4 v75, s[12:15], s72 offen offset:2048 lds\n"
    "ds_read_b128 a[176:179], v79\n"
    "v_mfma_f32_16x16x32_bf16 v[196:199], a[120:123], v[228:231], v[196:199]\n"
    "ds_read_b128 a[180:183], v79 offset:2048\n"
    "v_mfma_f32_16x16x32_bf16 v[200:203], a[124:127], v[228:231], v[200:203]\n"
    "ds_read_b64_tr_b16 v[228:229], v82 offset:9216\n"
    "s_waitcnt lgkmcnt(9)\n"
    "v_mfma_f32_16x16x32_bf16 v[208:211], v[60:63], v[232:235], v[208:211]\n"
    "buffer_load_dwordx4 v75, s[12:15], s72 offen offset:3072 lds\n"
    "ds_read_b64_tr_b16 v[230:231], v82 offset:9728\n"
    "ds_read_b128 a[184:187], v79 offset:4096\n"
    "ds_read_b128 a[188:191], v79 offset:6144\n"
    "v_mfma_f32_16x16x32_bf16 v[212:215], v[232:235], v[232:235], v[212:215]\n"
    "v_mfma_f32_16x16x32_bf16 v[208:211], v[60:63], v[92:95], v[208:211]\n"
    "v_mfma_f32_16x16x32_bf16 v[212:215], v[92:95], v[92:95], v[212:215]\n"
    "v_mfma_f32_16x16x32_bf16 v[184:187], v[216:219], v[60:63], v[184:187]\n"
    "v_mfma_f32_16x16x32_bf16 v[168:171], v[216:219], a[128:131], v[168:171]\n"
    "s_add_u32 m0, s85, 0x14000\n"
    "v_mfma_f32_16x16x32_bf16 v[172:175], v[216:219], a[132:135], v[172:175]\n"
    "buffer_load_dwordx4 v72, s[8:11], s65 offen lds\n"
    "v_mfma_f32_16x16x32_bf16 v[176:179], v[216:219], a[136:139], v[176:179]\n"
    "buffer_load_dwordx4 v73, s[8:11], s65 offen offset:1024 lds\n"
    "v_mfma_f32_16x16x32_bf16 v[180:183], v[216:219], a[140:143], v[180:183]\n"
    "s_add_u32 s65, s79, 0x51000\n"
    "v_mfma_f32_16x16x32_bf16 v[204:207], v[60:63], v[224:227], v[204:207]\n"
    "v_mfma_f32_16x16x32_bf16 v[188:191], a[128:131], v[224:227], v[188:191]\n"
    "v_mfma_f32_16x16x32_bf16 v[192:195], a[132:135], v[224:227], v[192:195]\n"
    "v_mfma_f32_16x16x32_bf16 v[196:199], a[136:139], v[224:227], v[196:199]\n"
    "s_add_u32 m0, s85, 0x22000\n"
    "v_mfma_f32_16x16x32_bf16 v[200:203], a[140:143], v[224:227], v[200:203]\n"
    "buffer_load_dwordx4 v74, s[12:15], s70 offen lds\n"
    "buffer_load_dwordx4 v75, s[12:15], s70 offen offset:1024 lds\n"
    "s_waitcnt lgkmcnt(0)\n"
    "v_mfma_f32_16x16x32_bf16 v[184:187], v[220:223], v[60:63], v[184:187]\n"
    "s_waitcnt vmcnt(10)\n"
    "s_barrier\n"
    "v_mfma_f32_16x16x32_bf16 v[168:171], v[220:223], a[144:147], v[168:171]\n"
    "ds_read_b128 v[232:235], v80 offset:8192\n"
    "ds_read_b128 v[92:95], v81 offset:8192\n"
    "v_mfma_f32_16x16x32_bf16 v[172:175], v[220:223], a[148:151], v[172:175]\n"
    "ds_read_b64_tr_b16 v[216:217], v82 offset:16384\n"
    "v_mfma_f32_16x16x32_bf16 v[176:179], v[220:223], a[152:155], v[176:179]\n"
    "ds_read_b64_tr_b16 v[218:219], v82 offset:16896\n"
    "ds_read_b64_tr_b16 v[224:225], v82 offset:17408\n"
    "v_mfma_f32_16x16x32_bf16 v[180:183], v[220:223], a[156:159], v[180:183]\n"
    "ds_read_b64_tr_b16 v[226:227], v82 offset:17920\n"
    "ds_read_b64_tr_b16 v[220:221], v82 offset:24576\n"
    "v_mfma_f32_16x16x32_bf16 v[204:207], v[60:63], v[228:231], v[204:207]\n"
    "ds_read_b64_tr_b16 v[222:223], v82 offset:25088\n"
    "ds_read_b128 a[192:195], v78 offset:8192\n"
    "v_mfma_f32_16x16x32_bf16 v[188:191], a[144:147], v[228:231], v[188:191]\n"
    "ds_read_b128 a[196:199], v78 offset:10240\n"
    "v_mfma_f32_16x16x32_bf16 v[192:195], a[148:151], v[228:231], v[192:195]\n"
    "ds_read_b128 a[200:203], v78 offset:12288\n"
    "v_mfma_f32_16x16x32_bf16 v[196:199], a[152:155], v[228:231], v[196:199]\n"
    "ds_read_b128 a[204:207], v78 offset:14336\n"
    "v_mfma_f32_16x16x32_bf16 v[200:203], a[156:159], v[228:231], v[200:203]\n"
    "s_add_u32 m0, s84, 0xc000\n"
    "ds_read_b64_tr_b16 v[228:229], v82 offset:25600\n"
    "buffer_load_dwordx4 v76, s[12:15], s73 offen lds\n"
    "ds_read_b64_tr_b16 v[230:231], v82 offset:26112\n"
    "buffer_load_dwordx4 v76, s[12:15], s73 offen offset:1024 lds\n"
    "ds_read_b128 a[208:211], v79 offset:8192\n"
    "ds_read_b128 a[212:215], v79 offset:10240\n"
    "s_waitcnt lgkmcnt(9)\n"
    "v_mfma_f32_16x16x32_bf16 v[208:211], v[60:63], v[232:235], v[208:211]\n"
    "ds_read_b128 a[216:219], v79 offset:12288\n"
    "v_mfma_f32_16x16x32_bf16 v[212:215], v[232:235], v[232:235], v[212:215]\n"
    "buffer_load_dwordx4 v75, s[12:15], s73 offen offset:2048 lds\n"
    "ds_read_b128 a[220:223], v79 offset:14336\n"
    "v_mfma_f32_16x16x32_bf16 v[208:211], v[60:63], v[92:95], v[208:211]\n"
    "v_mfma_f32_16x16x32_bf16 v[212:215], v[92:95], v[92:95], v[212:215]\n"
    "v_mfma_f32_16x16x32_bf16 v[184:187], v[216:219], v[60:63], v[184:187]\n"
    "v_mfma_f32_16x16x32_bf16 v[168:171], v[216:219], a[160:163], v[168:171]\n"
    "buffer_load_dwordx4 v75, s[12:15], s73 offen offset:3072 lds\n"
    "v_mfma_f32_16x16x32_bf16 v[172:175], v[216:219], a[164:167], v[172:175]\n"
    "v_mfma_f32_16x16x32_bf16 v[176:179], v[216:219], a[168:171], v[176:179]\n"
    "v_mfma_f32_16x16x32_bf16 v[180:183], v[216:219], a[172:175], v[180:183]\n"
    "v_mfma_f32_16x16x32_bf16 v[204:207], v[60:63], v[224:227], v[204:207]\n"
    "v_mfma_f32_16x16x32_bf16 v[188:191], a[160:163], v[224:227], v[188:191]\n"
    "v_mfma_f32_16x16x32_bf16 v[192:195], a[164:167], v[224:227], v[192:195]\n"
    "v_mfma_f32_16x16x32_bf16 v[196:199], a[168:171], v[224:227], v[196:199]\n"
    "v_mfma_f32_16x16x32_bf16 v[200:203], a[172:175], v[224:227], v[200:203]\n"
    "buffer_load_dword v28, v39, s[28:31], 0 offen sc1\n"
    "s_add_u32 m0, s85, 0x24000\n"
    "s_nop 0\n"
    "buffer_load_dwordx4 v74, s[12:15], s71 offen lds\n"
    "s_waitcnt lgkmcnt(0)\n"
    "v_mfma_f32_16x16x32_bf16 v[184:187], v[220:223], v[60:63], v[184:187]\n"
    "buffer_load_dwordx4 v75, s[12:15], s71 offen offset:1024 lds\n"
    "v_mfma_f32_16x16x32_bf16 v[168:171], v[220:223], a[176:179], v[168:171]\n"
    "s_waitcnt vmcnt(9)\n"
    "s_barrier\n"
    "ds_read_b128 v[232:235], v80 offset:16384\n"
    "ds_read_b128 v[92:95], v81 offset:16384\n"
    "v_mfma_f32_16x16x32_bf16 v[172:175], v[220:223], a[180:183], v[172:175]\n"
    "ds_read_b64_tr_b16 v[216:217], v82 offset:32768\n"
    "v_mfma_f32_16x16x32_bf16 v[176:179], v[220:223], a[184:187], v[176:179]\n"
    "ds_read_b64_tr_b16 v[218:219], v82 offset:33280\n"
    "ds_read_b64_tr_b16 v[224:225], v82 offset:33792\n"
    "v_mfma_f32_16x16x32_bf16 v[180:183], v[220:223], a[188:191], v[180:183]\n"
    "ds_read_b64_tr_b16 v[226:227], v82 offset:34304\n"
    "ds_read_b64_tr_b16 v[220:221], v82 offset:40960\n"
    "v_mfma_f32_16x16x32_bf16 v[204:207], v[60:63], v[228:231], v[204:207]\n"
    "ds_read_b64_tr_b16 v[222:223], v82 offset:41472\n"
    "ds_read_b128 a[224:227], v78 offset:16384\n"
    "v_mfma_f32_16x16x32_bf16 v[188:191], a[176:179], v[228:231], v[188:191]\n"
    "ds_read_b128 a[228:231], v78 offset:18432\n"
    "v_mfma_f32_16x16x32_bf16 v[192:195], a[180:183], v[228:231], v[192:195]\n"
    "ds_read_b128 a[232:235], v78 offset:20480\n"
    "v_mfma_f32_16x16x32_bf16 v[196:199], a[184:187], v[228:231], v[196:199]\n"
    "ds_read_b128 a[236:239], v78 offset:22528\n"
    "v_mfma_f32_16x16x32_bf16 v[200:203], a[188:191], v[228:231], v[200:203]\n"
    "ds_read_b64_tr_b16 v[228:229], v82 offset:41984\n"
    "s_waitcnt lgkmcnt(11)\n"
    "s_add_u32 m0, s85, 0x26000\n"
    "v_mfma_f32_16x16x32_bf16 v[208:211], v[60:63], v[232:235], v[208:211]\n"
    "buffer_load_dwordx4 v74, s[12:15], s72 offen lds\n"
    "ds_read_b64_tr_b16 v[230:231], v82 offset:42496\n"
    "v_mfma_f32_16x16x32_bf16 v[212:215], v[232:235], v[232:235], v[212:215]\n"
    "ds_read_b128 a[240:243], v79 offset:16384\n"
    "ds_read_b128 a[244:247], v79 offset:18432\n"
    "v_mfma_f32_16x16x32_bf16 v[208:211], v[60:63], v[92:95], v[208:211]\n"
    "ds_read_b128 a[248:251], v79 offset:20480\n"
    "v_mfma_f32_16x16x32_bf16 v[212:215], v[92:95], v[92:95], v[212:215]\n"
    "ds_read_b128 a[252:255], v79 offset:22528\n"
    "s_waitcnt lgkmcnt(8)\n"
    "v_mfma_f32_16x16x32_bf16 v[184:187], v[216:219], v[60:63], v[184:187]\n"
    "buffer_load_dwordx4 v75, s[12:15], s72 offen offset:1024 lds\n"
    "s_add_u32 m0, s85, 0x1a000\n"
    "v_mfma_f32_16x16x32_bf16 v[168:171], v[216:219], a[192:195], v[168:171]\n"
    "buffer_load_dwordx4 v74, s[12:15], s73 offen lds\n"
    "v_mfma_f32_16x16x32_bf16 v[172:175], v[216:219], a[196:199], v[172:175]\n"
    "v_cndmask_b32 v65, v212, v213, s[48:49]\n"
    "v_mul_f32 v68, 0x3b000000, v208\n"
    "v_mfma_f32_16x16x32_bf16 v[176:179], v[216:219], a[200:203], v[176:179]\n"
    "v_cndmask_b32 v65, v65, v214, s[50:51]\n"
    "v_cmp_le_i32 s[48:49], 2, v36\n"
    "v_mfma_f32_16x16x32_bf16 v[180:183], v[216:219], a[204:207], v[180:183]\n"
    "v_cndmask_b32 v65, v65, v215, s[52:53]\n"
    "v_cmp_le_i32 s[50:51], 3, v36\n"
    "v_mfma_f32_16x16x32_bf16 v[204:207], v[60:63], v[224:227], v[204:207]\n"
    "v_mul_f32 v66, 0x3b000000, v65\n"
    "v_cndmask_b32 v209, v37, 0, s[90:91]\n"
    "v_mfma_f32_16x16x32_bf16 v[188:191], a[192:195], v[224:227], v[188:191]\n"
    "v_fma_f32 v66, -v68, v68, v66\n"
    "v_cndmask_b32 v210, v37, 0, s[48:49]\n"
    "v_mfma_f32_16x16x32_bf16 v[192:195], a[196:199], v[224:227], v[192:195]\n"
    "v_max_f32 v66, 0, v66\n"
    "v_cndmask_b32 v211, v37, 0, s[50:51]\n"
    "v_mfma_f32_16x16x32_bf16 v[196:199], a[200:203], v[224:227], v[196:199]\n"
    "v_add_f32 v66, 0x3727c5ac, v66\n"
    "v_cmp_le_i32 s[90:91], 9, v36\n"
    "v_mfma_f32_16x16x32_bf16 v[200:203], a[204:207], v[224:227], v[200:203]\n"
    "v_rsq_f32 v66, v66\n"
    "v_mfma_f32_16x16x32_bf16 v[184:187], v[220:223], v[60:63], v[184:187]\n"
    "buffer_load_dwordx4 v75, s[12:15], s73 offen offset:1024 lds\n"
    "ds_write_b32 v85, v66\n"
    "v_mfma_f32_16x16x32_bf16 v[168:171], v[220:223], a[208:211], v[168:171]\n"
    "v_mul_f32_e64 v67, -v68, v66\n"
    "v_cmp_le_i32 s[48:49], 10, v36\n"
    "v_cmp_le_i32 s[50:51], 11, v36\n"
    "ds_write_b32 v85, v67 offset:256\n"
    "v_mfma_f32_16x16x32_bf16 v[172:175], v[220:223], a[212:215], v[172:175]\n"
    "s_waitcnt lgkmcnt(2)\n"
    "s_waitcnt vmcnt(7)\n"
    "s_barrier\n"
    "ds_read_b64_tr_b16 v[216:217], v82 offset:49152\n"
    "ds_read_b64_tr_b16 v[218:219], v82 offset:49664\n"
    "ds_read_b64_tr_b16 v[224:225], v82 offset:50176\n"
    "v_mfma_f32_16x16x32_bf16 v[176:179], v[220:223], a[216:219], v[176:179]\n"
    "ds_read_b64_tr_b16 v[226:227], v82 offset:50688\n"
    "v_cndmask_b32 v213, v37, 0, s[90:91]\n"
    "v_mfma_f32_16x16x32_bf16 v[180:183], v[220:223], a[220:223], v[180:183]\n"
    "ds_read_b64_tr_b16 v[220:221], v82 offset:57344\n"
    "ds_read_b64_tr_b16 v[222:223], v82 offset:57856\n"
    "v_mfma_f32_16x16x32_bf16 v[204:207], v[60:63], v[228:231], v[204:207]\n"
    "v_cndmask_b32 v214, v37, 0, s[48:49]\n"
    "v_cndmask_b32 v215, v37, 0, s[50:51]\n"
    "v_mfma_f32_16x16x32_bf16 v[188:191], a[208:211], v[228:231], v[188:191]\n"
    "v_cmp_le_i32 s[90:91], 17, v36\n"
    "v_cmp_le_i32 s[48:49], 18, v36\n"
    "v_mfma_f32_16x16x32_bf16 v[192:195], a[212:215], v[228:231], v[192:195]\n"
    "v_cmp_le_i32 s[50:51], 19, v36\n"
    "s_and_b32 s52, s74, 1\n"
    "v_mfma_f32_16x16x32_bf16 v[196:199], a[216:219], v[228:231], v[196:199]\n"
    "s_lshl_b32 s54, s52, 6\n"
    "s_lshr_b32 s53, s74, 1\n"
    "v_mfma_f32_16x16x32_bf16 v[200:203], a[220:223], v[228:231], v[200:203]\n"
    "ds_read_b64_tr_b16 v[228:229], v82 offset:58368\n"
    "ds_read_b64_tr_b16 v[230:231], v82 offset:58880\n"
    "s_waitcnt lgkmcnt(5)\n"
    "s_add_u32 m0, s85, 0x12000\n"
    "v_mfma_f32_16x16x32_bf16 v[184:187], v[216:219], v[60:63], v[184:187]\n"
    "buffer_load_dwordx4 v72, s[8:11], s94 offen lds\n"
    "v_mfma_f32_16x16x32_bf16 v[168:171], v[216:219], a[224:227], v[168:171]\n"
    "buffer_load_dwordx4 v73, s[8:11], s94 offen offset:1024 lds\n"
    "v_mfma_f32_16x16x32_bf16 v[172:175], v[216:219], a[228:231], v[172:175]\n"
    "s_lshl_b32 s53, s53, 10\n"
    "s_mov_b32 s8, s6\n"
    "v_mfma_f32_16x16x32_bf16 v[176:179], v[216:219], a[232:235], v[176:179]\n"
    "s_and_b32 s9, s7, 0xffff\n"
    "s_mov_b32 s10, 0x400\n"
    "v_mfma_f32_16x16x32_bf16 v[180:183], v[216:219], a[236:239], v[180:183]\n"
    "v_cndmask_b32 v217, v37, 0, s[90:91]\n"
    "v_cndmask_b32 v218, v37, 0, s[48:49]\n"
    "s_waitcnt lgkmcnt(1)\n"
    "v_mfma_f32_16x16x32_bf16 v[204:207], v[60:63], v[224:227], v[204:207]\n"
    "v_cndmask_b32 v219, v37, 0, s[50:51]\n"
    "s_waitcnt vmcnt(8)\n"
    "v_xor_b32 v28, v27, v28\n"
    "v_mfma_f32_16x16x32_bf16 v[188:191], a[224:227], v[224:227], v[188:191]\n"
    "v_lshrrev_b32 v29, v38, v28\n"
    "v_cmp_le_i32 s[90:91], 25, v36\n"
    "v_mfma_f32_16x16x32_bf16 v[192:195], a[228:231], v[224:227], v[192:195]\n"
    "v_cmp_ne_u32 vcc, 0, v29\n"
    "s_nop 1\n"
    "s_cbranch_vccnz L_pspin_%=\n"
    "L_pok_%=:\n"
    "v_mfma_f32_16x16x32_bf16 v[196:199], a[232:235], v[224:227], v[196:199]\n"
    "v_add_u32_dpp v28, v28, v28 quad_perm:[1,0,3,2] row_mask:0xf bank_mask:0xf\n"
    "v_cmp_le_i32 s[48:49], 26, v36\n"
    "v_mfma_f32_16x16x32_bf16 v[200:203], a[236:239], v[224:227], v[200:203]\n"
    "v_add_u32_dpp v28, v28, v28 quad_perm:[2,3,0,1] row_mask:0xf bank_mask:0xf\n"
    "v_cmp_le_i32 s[50:51], 27, v36\n"
    "v_mfma_f32_16x16x32_bf16 v[184:187], v[220:223], v[60:63], v[184:187]\n"
    "v_add_u32_dpp v28, v28, v28 row_half_mirror row_mask:0xf bank_mask:0xf\n"
    "s_barrier\n"
    "ds_read_b128 v[144:147], v87 offset:256\n"
    "ds_read_b128 v[160:163], v87 offset:320\n"
    "v_mfma_f32_16x16x32_bf16 v[168:171], v[220:223], a[240:243], v[168:171]\n"
    "ds_read_b128 v[136:139], v87\n"
    "v_readlane_b32 s88, v28, 0\n"
    "v_mfma_f32_16x16x32_bf16 v[172:175], v[220:223], a[244:247], v[172:175]\n"
    "ds_read_b128 v[152:155], v87 offset:64\n"
    "v_readlane_b32 s89, v28, 8\n"
    "v_mfma_f32_16x16x32_bf16 v[176:179], v[220:223], a[248:251], v[176:179]\n"
    "ds_read2_b32 v[244:245], v86 offset0:0 offset1:64\n"
    "s_cmp_gt_u32 s88, s89\n"
    "s_cselect_b32 s87, 0x2000, 0\n"
    "v_mfma_f32_16x16x32_bf16 v[180:183], v[220:223], a[252:255], v[180:183]\n"
    "ds_read2_b32 v[246:247], v86 offset0:16 offset1:80\n"
    "s_add_u32 s28, s28, s87\n"
    "s_waitcnt lgkmcnt(6)\n"
    "v_mfma_f32_16x16x32_bf16 v[204:207], v[60:63], v[228:231], v[204:207]\n"
    "ds_read2_b32 v[248:249], v86 offset0:32 offset1:96\n"
    "v_cmp_le_i32 s[88:89], 0, v36\n"
    "ds_read2_b32 v[250:251], v86 offset0:48 offset1:112\n"
    "v_mfma_f32_16x16x32_bf16 v[188:191], a[240:243], v[228:231], v[188:191]\n"
    "v_cndmask_b32 v208, v37, 0, s[88:89]\n"
    "v_cmp_le_i32 s[88:89], 8, v36\n"
    "v_mfma_f32_16x16x32_bf16 v[192:195], a[244:247], v[228:231], v[192:195]\n"
    "s_waitcnt lgkmcnt(7)\n"
    "v_pk_mul_f32 v[96:97], v[144:145], v[204:205]\n"
    "v_pk_mul_f32 v[98:99], v[146:147], v[204:205]\n"
    "ds_read_b128 v[144:147], v87 offset:384\n"
    "s_waitcnt lgkmcnt(6)\n"
    "v_pk_mul_f32 v[100:101], v[160:161], v[204:205]\n"
    "v_pk_mul_f32 v[102:103], v[162:163], v[204:205]\n"
    "ds_read_b128 v[160:163], v87 offset:448\n"
    "v_pk_fma_f32 v[96:97], v[188:189], v[136:137], v[96:97]\n"
    "v_pk_fma_f32 v[98:99], v[190:191], v[138:139], v[98:99]\n"
    "ds_read_b128 v[136:139], v87 offset:128\n"
    "s_waitcnt lgkmcnt(6)\n"
    "v_pk_fma_f32 v[100:101], v[192:193], v[152:153], v[100:101]\n"
    "v_pk_fma_f32 v[102:103], v[194:195], v[154:155], v[102:103]\n"
    "ds_read_b128 v[152:155], v87 offset:192\n"
    "v_mfma_f32_16x16x32_bf16 v[196:199], a[248:251], v[228:231], v[196:199]\n"
    "v_cvt_pk_bf16_f32 v48, v96, v97\n"
    "v_cvt_pk_bf16_f32 v49, v98, v99\n"
    "v_mfma_f32_16x16x32_bf16 v[200:203], a[252:255], v[228:231], v[200:203]\n"
    "v_cvt_pk_bf16_f32 v50, v100, v101\n"
    "v_cvt_pk_bf16_f32 v51, v102, v103\n"
    "v_pk_mul_f32 v[64:65], v[184:185], v[244:245] op_sel:[0,1] op_sel_hi:[1,1]\n"
    "v_pk_mul_f32 v[66:67], v[186:187], v[244:245] op_sel:[0,1] op_sel_hi:[1,1]\n"
    "s_waitcnt lgkmcnt(4)\n"
    "v_pk_mul_f32 v[68:69], v[184:185], v[246:247] op_sel:[0,1] op_sel_hi:[1,1]\n"
    "v_pk_mul_f32 v[70:71], v[186:187], v[246:247] op_sel:[0,1] op_sel_hi:[1,1]\n"
    "s_waitcnt lgkmcnt(3)\n"
    "v_pk_mul_f32 v[96:97], v[144:145], v[204:205]\n"
    "v_pk_mul_f32 v[98:99], v[146:147], v[204:205]\n"
    "s_waitcnt lgkmcnt(2)\n"
    "v_pk_mul_f32 v[100:101], v[160:161], v[204:205]\n"
    "v_pk_mul_f32 v[102:103], v[162:163], v[204:205]\n"
    "v_pk_fma_f32 v[64:65], v[168:169], v[244:245], v[64:65] op_sel_hi:[1,0,1]\n"
    "v_pk_fma_f32 v[66:67], v[170:171], v[244:245], v[66:67] op_sel_hi:[1,0,1]\n"
    "v_pk_fma_f32 v[68:69], v[172:173], v[246:247], v[68:69] op_sel_hi:[1,0,1]\n"
    "v_pk_fma_f32 v[70:71], v[174:175], v[246:247], v[70:71] op_sel_hi:[1,0,1]\n"
    "s_waitcnt lgkmcnt(1)\n"
    "v_pk_fma_f32 v[96:97], v[196:197], v[136:137], v[96:97]\n"
    "v_pk_fma_f32 v[98:99], v[198:199], v[138:139], v[98:99]\n"
    "s_waitcnt lgkmcnt(0)\n"
    "v_pk_fma_f32 v[100:101], v[200:201], v[152:153], v[100:101]\n"
    "v_pk_fma_f32 v[102:103], v[202:203], v[154:155], v[102:103]\n"
    "v_cvt_pk_bf16_f32 v40, v64, v65\n"
    "v_cvt_pk_bf16_f32 v41, v66, v67\n"
    "v_cvt_pk_bf16_f32 v42, v68, v69\n"
    "v_cvt_pk_bf16_f32 v43, v70, v71\n"
    "v_cvt_pk_bf16_f32 v56, v96, v97\n"
    "v_cvt_pk_bf16_f32 v57, v98, v99\n"
    "v_cvt_pk_bf16_f32 v58, v100, v101\n"
    "v_cvt_pk_bf16_f32 v59, v102, v103\n"
    "v_pk_mul_f32 v[64:65], v[184:185], v[248:249] op_sel:[0,1] op_sel_hi:[1,1]\n"
    "v_pk_mul_f32 v[66:67], v[186:187], v[248:249] op_sel:[0,1] op_sel_hi:[1,1]\n"
    "v_pk_mul_f32 v[68:69], v[184:185], v[250:251] op_sel:[0,1] op_sel_hi:[1,1]\n"
    "v_pk_mul_f32 v[70:71], v[186:187], v[250:251] op_sel:[0,1] op_sel_hi:[1,1]\n"
    "v_pk_fma_f32 v[64:65], v[176:177], v[248:249], v[64:65] op_sel_hi:[1,0,1]\n"
    "v_pk_fma_f32 v[66:67], v[178:179], v[248:249], v[66:67] op_sel_hi:[1,0,1]\n"
    "v_pk_fma_f32 v[68:69], v[180:181], v[250:251], v[68:69] op_sel_hi:[1,0,1]\n"
    "v_pk_fma_f32 v[70:71], v[182:183], v[250:251], v[70:71] op_sel_hi:[1,0,1]\n"
    "v_permlane16_swap_b32 v48, v50\n"
    "v_permlane16_swap_b32 v49, v51\n"
    "v_permlane16_swap_b32 v56, v58\n"
    "v_permlane16_swap_b32 v57, v59\n"
    "s_mov_b32 s11, 0x20000\n"
    "v_cvt_pk_bf16_f32 v44, v64, v65\n"
    "v_cvt_pk_bf16_f32 v45, v66, v67\n"
    "v_cvt_pk_bf16_f32 v46, v68, v69\n"
    "v_cvt_pk_bf16_f32 v47, v70, v71\n"
    "v_permlane16_swap_b32 v40, v42\n"
    "v_permlane16_swap_b32 v41, v43\n"
    "buffer_store_dwordx4 v[40:43], v1, s[28:31], s5 offen sc1\n"
    "v_permlane16_swap_b32 v44, v46\n"
    "v_permlane16_swap_b32 v45, v47\n"
    "v_permlane32_swap_b32 v48, v50\n"
    "v_cndmask_b32 v212, v37, 0, s[88:89]\n"
    "buffer_store_dwordx4 v[44:47], v1, s[28:31], s5 offen offset:1024 sc1\n"
    "v_mul_f32 v244, 0x3eb8aa3b, v244\n"
    "v_permlane32_swap_b32 v49, v51\n"
    "buffer_store_dwordx4 v[48:51], v1, s[28:31], s5 offen offset:2048 sc1\n"
    "v_permlane32_swap_b32 v56, v58\n"
    "v_permlane32_swap_b32 v57, v59\n"
    "v_mfma_f32_16x16x32_bf16 v[240:243], v[252:255], v[60:63], 0\n"
    "v_mul_f32 v245, 0x3eb8aa3b, v245\n"
    "v_mul_f32 v246, 0x3eb8aa3b, v246\n"
    "v_mfma_f32_16x16x32_bf16 v[104:107], v[252:255], a[0:3], 0\n"
    "v_mul_f32 v247, 0x3eb8aa3b, v247\n"
    "v_cmp_le_i32 s[88:89], 16, v36\n"
    "v_cndmask_b32 v221, v37, 0, s[90:91]\n"
    "buffer_store_dwordx4 v[56:59], v1, s[28:31], s5 offen offset:3072 sc1\n"
    "v_mfma_f32_16x16x32_bf16 v[112:115], v[252:255], a[4:7], 0\n"
    "s_waitcnt vmcnt(4)\n"
    "s_barrier\n"
    "buffer_load_dword v16, v88, s[16:19], 0 offen\n"
    "ds_read_b64_tr_b16 v[176:177], v83 offset:49152\n"
    "ds_read_b64_tr_b16 v[178:179], v83 offset:49664\n"
    "ds_read_b64_tr_b16 v[180:181], v83 offset:53248\n"
    "buffer_load_dword v16, v89, s[20:23], 0 offen offset:2048\n"
    "ds_read_b64_tr_b16 v[182:183], v83 offset:53760\n"
    "ds_read_b64_tr_b16 v[168:169], v83 offset:57344\n"
    "ds_read_b64_tr_b16 v[170:171], v83 offset:57856\n"
    "ds_read_b64_tr_b16 v[172:173], v83 offset:61440\n"
    "buffer_load_dword v16, v90, s[24:27], 0 offen\n"
    "ds_read_b64_tr_b16 v[174:175], v83 offset:61952\n"
    "ds_read_b64 v[8:9], v84\n"
    "v_mfma_f32_16x16x32_bf16 v[120:123], v[252:255], a[8:11], 0\n"
    "ds_read_b64 v[10:11], v84 offset:2048\n"
    "ds_read_b64 v[12:13], v84 offset:4096\n"
    "buffer_load_dword v16, v91, s[28:31], s3 offen offset:128 sc1\n"
    "ds_read_b64 v[14:15], v84 offset:6144\n"
    "v_mfma_f32_16x16x32_bf16 v[128:131], v[252:255], a[12:15], 0\n"
    "ds_read_b64_tr_b16 v[252:253], v83 offset:16384\n"
    "ds_read_b64_tr_b16 v[254:255], v83 offset:16896\n"
    "v_mfma_f32_16x16x32_bf16 v[240:243], v[236:239], v[60:63], v[240:243]\n"
    "v_cndmask_b32 v216, v37, 0, s[88:89]\n"
    "v_cmp_le_i32 s[88:89], 24, v36\n"
    "v_mfma_f32_16x16x32_bf16 v[104:107], v[236:239], a[16:19], v[104:107]\n"
    "v_cndmask_b32 v222, v37, 0, s[48:49]\n"
    "v_cndmask_b32 v220, v37, 0, s[88:89]\n"
    "v_mfma_f32_16x16x32_bf16 v[112:115], v[236:239], a[20:23], v[112:115]\n"
    "v_cndmask_b32 v223, v37, 0, s[50:51]\n"
    "v_mul_f32 v248, 0x3eb8aa3b, v248\n"
    "v_mfma_f32_16x16x32_bf16 v[120:123], v[236:239], a[24:27], v[120:123]\n"
    "v_mul_f32 v249, 0x3eb8aa3b, v249\n"
    "v_mul_f32 v250, 0x3eb8aa3b, v250\n"
    "v_mfma_f32_16x16x32_bf16 v[128:131], v[236:239], a[28:31], v[128:131]\n"
    "ds_read_b64_tr_b16 v[236:237], v83 offset:20480\n"
    "ds_read_b64_tr_b16 v[238:239], v83 offset:20992\n"
    "v_mul_f32 v251, 0x3eb8aa3b, v251\n"
    "v_permlane16_swap_b32 v48, v52\n"
    "s_waitcnt lgkmcnt(2)\n"
    "v_mfma_f32_16x16x32_bf16 v[240:243], v[252:255], v[60:63], v[240:243]\n"
    "v_permlane16_swap_b32 v49, v53\n"
    "v_mfma_f32_16x16x32_bf16 v[104:107], v[252:255], a[32:35], v[104:107]\n"
    "v_permlane16_swap_b32 v50, v54\n"
    "v_mfma_f32_16x16x32_bf16 v[112:115], v[252:255], a[36:39], v[112:115]\n"
    "v_permlane16_swap_b32 v51, v55\n"
    "v_mfma_f32_16x16x32_bf16 v[120:123], v[252:255], a[40:43], v[120:123]\n"
    "s_lshl_b32 s88, s76, 10\n"
    "s_add_u32 s93, s88, s79\n"
    "v_mfma_f32_16x16x32_bf16 v[128:131], v[252:255], a[44:47], v[128:131]\n"
    "ds_read_b64_tr_b16 v[252:253], v83 offset:24576\n"
    "ds_read_b64_tr_b16 v[254:255], v83 offset:25088\n"
    "s_waitcnt lgkmcnt(2)\n"
    "v_mfma_f32_16x16x32_bf16 v[240:243], v[236:239], v[60:63], v[240:243]\n"
    "s_add_u32 s92, s93, 0x40000\n"
    "s_add_u32 s94, s92, 0x10000\n"
    "v_mfma_f32_16x16x32_bf16 v[104:107], v[236:239], a[48:51], v[104:107]\n"
    "v_mfma_f32_16x16x32_bf16 v[112:115], v[236:239], a[52:55], v[112:115]\n"
    "v_mfma_f32_16x16x32_bf16 v[120:123], v[236:239], a[56:59], v[120:123]\n"
    "v_mfma_f32_16x16x32_bf16 v[128:131], v[236:239], a[60:63], v[128:131]\n"
    "ds_read_b64_tr_b16 v[236:237], v83 offset:28672\n"
    "ds_read_b64_tr_b16 v[238:239], v83 offset:29184\n"
    "s_waitcnt lgkmcnt(2)\n"
    "v_mfma_f32_16x16x32_bf16 v[240:243], v[252:255], v[60:63], v[240:243]\n"
    "v_mfma_f32_16x16x32_bf16 v[104:107], v[252:255], a[64:67], v[104:107]\n"
    "v_mfma_f32_16x16x32_bf16 v[112:115], v[252:255], a[68:71], v[112:115]\n"
    "v_mfma_f32_16x16x32_bf16 v[120:123], v[252:255], a[72:75], v[120:123]\n"
    "v_mfma_f32_16x16x32_bf16 v[128:131], v[252:255], a[76:79], v[128:131]\n"
    "ds_read_b64_tr_b16 v[252:253], v83 offset:32768\n"
    "ds_read_b64_tr_b16 v[254:255], v83 offset:33280\n"
    "s_waitcnt lgkmcnt(2)\n"
    "v_mfma_f32_16x16x32_bf16 v[240:243], v[236:239], v[60:63], v[240:243]\n"
    "v_mfma_f32_16x16x32_bf16 v[104:107], v[236:239], a[80:83], v[104:107]\n"
    "v_mfma_f32_16x16x32_bf16 v[112:115], v[236:239], a[84:87], v[112:115]\n"
    "v_mfma_f32_16x16x32_bf16 v[120:123], v[236:239], a[88:91], v[120:123]\n"
    "v_mfma_f32_16x16x32_bf16 v[128:131], v[236:239], a[92:95], v[128:131]\n"
    "ds_read_b64_tr_b16 v[236:237], v83 offset:36864\n"
    "ds_read_b64_tr_b16 v[238:239], v83 offset:37376\n"
    "s_waitcnt lgkmcnt(2)\n"
    "v_mfma_f32_16x16x32_bf16 v[240:243], v[252:255], v[60:63], v[240:243]\n"
    "v_mfma_f32_16x16x32_bf16 v[104:107], v[252:255], a[96:99], v[104:107]\n"
    "v_mfma_f32_16x16x32_bf16 v[112:115], v[252:255], a[100:103], v[112:115]\n"
    "v_mfma_f32_16x16x32_bf16 v[120:123], v[252:255], a[104:107], v[120:123]\n"
    "v_mfma_f32_16x16x32_bf16 v[128:131], v[252:255], a[108:111], v[128:131]\n"
    "ds_read_b64_tr_b16 v[252:253], v83 offset:40960\n"
    "ds_read_b64_tr_b16 v[254:255], v83 offset:41472\n"
    "s_waitcnt lgkmcnt(2)\n"
    "v_mfma_f32_16x16x32_bf16 v[240:243], v[236:239], v[60:63], v[240:243]\n"
    "v_mfma_f32_16x16x32_bf16 v[104:107], v[236:239], a[112:115], v[104:107]\n"
    "v_mfma_f32_16x16x32_bf16 v[112:115], v[236:239], a[116:119], v[112:115]\n"
    "v_mfma_f32_16x16x32_bf16 v[120:123], v[236:239], a[120:123], v[120:123]\n"
    "v_mfma_f32_16x16x32_bf16 v[128:131], v[236:239], a[124:127], v[128:131]\n"
    "ds_read_b64_tr_b16 v[236:237], v83 offset:45056\n"
    "ds_read_b64_tr_b16 v[238:239], v83 offset:45568\n"
    "s_waitcnt lgkmcnt(2)\n"
    "v_mfma_f32_16x16x32_bf16 v[240:243], v[252:255], v[60:63], v[240:243]\n"
    "v_mfma_f32_16x16x32_bf16 v[104:107], v[252:255], a[128:131], v[104:107]\n"
    "v_mfma_f32_16x16x32_bf16 v[112:115], v[252:255], a[132:135], v[112:115]\n"
    "v_mfma_f32_16x16x32_bf16 v[120:123], v[252:255], a[136:139], v[120:123]\n"
    "v_mfma_f32_16x16x32_bf16 v[128:131], v[252:255], a[140:143], v[128:131]\n"
    "s_waitcnt lgkmcnt(0)\n"
    "v_mfma_f32_16x16x32_bf16 v[240:243], v[236:239], v[60:63], v[240:243]\n"
    "v_mfma_f32_16x16x32_bf16 v[104:107], v[236:239], a[144:147], v[104:107]\n"
    "v_mfma_f32_16x16x32_bf16 v[112:115], v[236:239], a[148:151], v[112:115]\n"
    "v_mfma_f32_16x16x32_bf16 v[120:123], v[236:239], a[152:155], v[120:123]\n"
    "v_mfma_f32_16x16x32_bf16 v[128:131], v[236:239], a[156:159], v[128:131]\n"
    "v_mfma_f32_16x16x32_bf16 v[240:243], v[176:179], v[60:63], v[240:243]\n"
    "v_mfma_f32_16x16x32_bf16 v[104:107], v[176:179], a[160:163], v[104:107]\n"
    "v_mfma_f32_16x16x32_bf16 v[112:115], v[176:179], a[164:167], v[112:115]\n"
    "s_waitcnt vmcnt(4)\n"
    "buffer_store_dword v26, v30, s[28:31], s3 offen sc1\n"
    "v_mov_b32 v28, 0\n"
    "v_mfma_f32_16x16x32_bf16 v[120:123], v[176:179], a[168:171], v[120:123]\n"
    "v_add_u32 v30, 0x5400, v6\n"
    "v_mfma_f32_16x16x32_bf16 v[128:131], v[176:179], a[172:175], v[128:131]\n"
    "ds_read_b64_tr_b16 v[176:177], v83 offset:8192\n"
    "ds_read_b64_tr_b16 v[178:179], v83 offset:8704\n"
    "v_mfma_f32_16x16x32_bf16 v[240:243], v[180:183], v[60:63], v[240:243]\n"
    "v_mfma_f32_16x16x32_bf16 v[104:107], v[180:183], a[176:179], v[104:107]\n"
    "v_mfma_f32_16x16x32_bf16 v[112:115], v[180:183], a[180:183], v[112:115]\n"
    "v_mfma_f32_16x16x32_bf16 v[120:123], v[180:183], a[184:187], v[120:123]\n"
    "v_mfma_f32_16x16x32_bf16 v[128:131], v[180:183], a[188:191], v[128:131]\n"
    "ds_read_b64_tr_b16 v[180:181], v83 offset:12288\n"
    "ds_read_b64_tr_b16 v[182:183], v83 offset:12800\n"
    "v_mfma_f32_16x16x32_bf16 v[240:243], v[168:171], v[60:63], v[240:243]\n"
    "v_mfma_f32_16x16x32_bf16 v[104:107], v[168:171], a[192:195], v[104:107]\n"
    "v_mfma_f32_16x16x32_bf16 v[112:115], v[168:171], a[196:199], v[112:115]\n"
    "v_mfma_f32_16x16x32_bf16 v[120:123], v[168:171], a[200:203], v[120:123]\n"
    "v_mfma_f32_16x16x32_bf16 v[128:131], v[168:171], a[204:207], v[128:131]\n"
    "v_mfma_f32_16x16x32_bf16 v[240:243], v[172:175], v[60:63], v[240:243]\n"
    "s_waitcnt lgkmcnt(0)\n"
    "s_barrier\n"
    "v_mfma_f32_16x16x32_bf16 v[104:107], v[172:175], a[208:211], v[104:107]\n"
    "v_mfma_f32_16x16x32_bf16 v[112:115], v[172:175], a[212:215], v[112:115]\n"
    "v_mfma_f32_16x16x32_bf16 v[120:123], v[172:175], a[216:219], v[120:123]\n"
    "v_mfma_f32_16x16x32_bf16 v[128:131], v[172:175], a[220:223], v[128:131]\n"
    "v_mfma_f32_16x16x32_bf16 v[240:243], v[176:179], v[60:63], v[240:243]\n"
    "v_mfma_f32_16x16x32_bf16 v[104:107], v[176:179], a[224:227], v[104:107]\n"
    "v_mfma_f32_16x16x32_bf16 v[112:115], v[176:179], a[228:231], v[112:115]\n"
    "v_mfma_f32_16x16x32_bf16 v[120:123], v[176:179], a[232:235], v[120:123]\n"
    "v_mfma_f32_16x16x32_bf16 v[128:131], v[176:179], a[236:239], v[128:131]\n"
    "v_mfma_f32_16x16x32_bf16 v[240:243], v[180:183], v[60:63], v[240:243]\n"
    "v_permlane16_swap_b32 v56, v60\n"
    "v_mfma_f32_16x16x32_bf16 v[104:107], v[180:183], a[240:243], v[104:107]\n"
    "v_permlane16_swap_b32 v57, v61\n"
    "v_mfma_f32_16x16x32_bf16 v[112:115], v[180:183], a[244:247], v[112:115]\n"
    "s_nop 3\n"
    "v_pk_mul_f32 v[224:225], v[240:241], v[244:245] op_sel:[0,1] op_sel_hi:[1,1]\n"
    "v_pk_mul_f32 v[226:227], v[242:243], v[244:245] op_sel:[0,1] op_sel_hi:[1,1]\n"
    "s_nop 0\n"
    "v_mfma_f32_16x16x32_bf16 v[120:123], v[180:183], a[248:251], v[120:123]\n"
    "v_pk_mul_f32 v[228:229], v[240:241], v[246:247] op_sel:[0,1] op_sel_hi:[1,1]\n"
    "v_pk_mul_f32 v[230:231], v[242:243], v[246:247] op_sel:[0,1] op_sel_hi:[1,1]\n"
    "v_pk_fma_f32 v[224:225], v[104:105], v[244:245], v[224:225] op_sel_hi:[1,0,1]\n"
    "v_pk_fma_f32 v[226:227], v[106:107], v[244:245], v[226:227] op_sel_hi:[1,0,1]\n"
    "v_pk_fma_f32 v[228:229], v[112:113], v[246:247], v[228:229] op_sel_hi:[1,0,1]\n"
    "v_pk_fma_f32 v[230:231], v[114:115], v[246:247], v[230:231] op_sel_hi:[1,0,1]\n"
    "v_cvt_pk_bf16_f32 v64, v224, v225\n"
    "v_cvt_pk_bf16_f32 v65, v226, v227\n"
    "v_cvt_pk_bf16_f32 v66, v228, v229\n"
    "v_cvt_pk_bf16_f32 v67, v230, v231\n"
    "v_mfma_f32_16x16x32_bf16 v[128:131], v[180:183], a[252:255], v[128:131]\n"
    "v_permlane16_swap_b32 v64, v66\n"
    "v_permlane16_swap_b32 v65, v67\n"
    "v_pk_mul_f32 v[224:225], v[240:241], v[248:249] op_sel:[0,1] op_sel_hi:[1,1]\n"
    "v_pk_mul_f32 v[226:227], v[242:243], v[248:249] op_sel:[0,1] op_sel_hi:[1,1]\n"
    "v_pk_mul_f32 v[228:229], v[240:241], v[250:251] op_sel:[0,1] op_sel_hi:[1,1]\n"
    "v_pk_mul_f32 v[230:231], v[242:243], v[250:251] op_sel:[0,1] op_sel_hi:[1,1]\n"
    "s_nop 0\n"
    "v_mfma_f32_32x32x16_bf16 v[168:183], v[40:43], v[64:67], v[208:223]\n"
    "v_pk_fma_f32 v[224:225], v[120:121], v[248:249], v[224:225] op_sel_hi:[1,0,1]\n"
    "v_pk_fma_f32 v[226:227], v[122:123], v[248:249], v[226:227] op_sel_hi:[1,0,1]\n"
    "v_pk_fma_f32 v[228:229], v[128:129], v[250:251], v[228:229] op_sel_hi:[1,0,1]\n"
    "v_pk_fma_f32 v[230:231], v[130:131], v[250:251], v[230:231] op_sel_hi:[1,0,1]\n"
    "v_cvt_pk_bf16_f32 v68, v224, v225\n"
    "v_cvt_pk_bf16_f32 v69, v226, v227\n"
    "v_cvt_pk_bf16_f32 v70, v228, v229\n"
    "v_cvt_pk_bf16_f32 v71, v230, v231\n"
    "v_permlane16_swap_b32 v58, v62\n"
    "v_permlane16_swap_b32 v68, v70\n"
    "v_permlane16_swap_b32 v69, v71\n"
    "v_permlane16_swap_b32 v59, v63\n"
    "s_nop 0\n"
    "v_mfma_f32_32x32x16_bf16 v[184:199], v[44:47], v[68:71], v[208:223]\n"
    "v_exp_f32 v168, v168\n"
    "v_exp_f32 v169, v169\n"
    "v_exp_f32 v170, v170\n"
    "v_exp_f32 v171, v171\n"
    "v_exp_f32 v172, v172\n"
    "v_exp_f32 v173, v173\n"
    "v_exp_f32 v174, v174\n"
    "v_exp_f32 v175, v175\n"
    "v_exp_f32 v176, v176\n"
    "v_cvt_pk_bf16_f32 v200, v168, v169\n"
    "v_cvt_pk_bf16_f32 v201, v170, v171\n"
    "buffer_atomic_or v28, v32, s[28:31], 0 offen sc0 sc1\n"
    "v_cvt_pk_bf16_f32 v202, v172, v173\n"
    "v_cvt_pk_bf16_f32 v203, v174, v175\n"
    "v_exp_f32 v177, v177\n"
    "v_exp_f32 v178, v178\n"
    "v_mfma_f32_32x32x16_bf16 v[224:239], v[48:51], v[200:203], 0\n"
    "v_exp_f32 v179, v179\n"
    "v_exp_f32 v180, v180\n"
    "v_exp_f32 v181, v181\n"
    "v_exp_f32 v182, v182\n"
    "v_exp_f32 v183, v183\n"
    "v_cvt_pk_bf16_f32 v204, v176, v177\n"
    "v_cvt_pk_bf16_f32 v205, v178, v179\n"
    "v_cvt_pk_bf16_f32 v206, v180, v181\n"
    "v_cvt_pk_bf16_f32 v207, v182, v183\n"
    "v_mfma_f32_32x32x16_bf16 v[168:183], v[40:43], v[68:71], 0\n"
    "v_exp_f32 v184, v184\n"
    "v_exp_f32 v185, v185\n"
    "v_exp_f32 v186, v186\n"
    "v_mfma_f32_32x32x16_bf16 v[224:239], v[52:55], v[204:207], v[224:239]\n"
    "v_exp_f32 v187, v187\n"
    "v_exp_f32 v188, v188\n"
    "v_exp_f32 v189, v189\n"
    "v_exp_f32 v190, v190\n"
    "v_exp_f32 v191, v191\n"
    "v_exp_f32 v192, v192\n"
    "v_cvt_pk_bf16_f32 v200, v184, v185\n"
    "v_cvt_pk_bf16_f32 v201, v186, v187\n"
    "v_cvt_pk_bf16_f32 v202, v188, v189\n"
    "v_cvt_pk_bf16_f32 v203, v190, v191\n"
    "v_exp_f32 v193, v193\n"
    "v_exp_f32 v194, v194\n"
    "v_mfma_f32_32x32x16_bf16 v[240:255], v[56:59], v[200:203], 0\n"
    "v_exp_f32 v195, v195\n"
    "v_exp_f32 v196, v196\n"
    "v_exp_f32 v197, v197\n"
    "v_exp_f32 v198, v198\n"
    "v_exp_f32 v199, v199\n"
    "v_cvt_pk_bf16_f32 v204, v192, v193\n"
    "v_cvt_pk_bf16_f32 v205, v194, v195\n"
    "v_cvt_pk_bf16_f32 v206, v196, v197\n"
    "v_cvt_pk_bf16_f32 v207, v198, v199\n"
    "v_exp_f32 v168, v168\n"
    "v_exp_f32 v169, v169\n"
    "v_exp_f32 v170, v170\n"
    "v_mfma_f32_32x32x16_bf16 v[240:255], v[60:63], v[204:207], v[240:255]\n"
    "v_exp_f32 v171, v171\n"
    "v_exp_f32 v172, v172\n"
    "v_exp_f32 v173, v173\n"
    "v_exp_f32 v174, v174\n"
    "v_exp_f32 v175, v175\n"
    "v_exp_f32 v176, v176\n"
    "v_cvt_pk_bf16_f32 v200, v168, v169\n"
    "v_cvt_pk_bf16_f32 v201, v170, v171\n"
    "v_cvt_pk_bf16_f32 v202, v172, v173\n"
    "v_cvt_pk_bf16_f32 v203, v174, v175\n"
    "v_exp_f32 v177, v177\n"
    "v_exp_f32 v178, v178\n"
    "v_mfma_f32_32x32x16_bf16 v[240:255], v[48:51], v[200:203], v[240:255]\n"
    "v_exp_f32 v179, v179\n"
    "v_exp_f32 v180, v180\n"
    "v_exp_f32 v181, v181\n"
    "v_exp_f32 v182, v182\n"
    "v_exp_f32 v183, v183\n"
    "v_cvt_pk_bf16_f32 v204, v176, v177\n"
    "v_cvt_pk_bf16_f32 v205, v178, v179\n"
    "v_cvt_pk_bf16_f32 v206, v180, v181\n"
    "v_cvt_pk_bf16_f32 v207, v182, v183\n"
    "v_sub_f32 v208, v37, v208\n"
    "s_waitcnt vmcnt(0)\n"
    "v_cmp_eq_u32 vcc, v28, v27\n"
    "s_nop 0\n"
    "s_or_b64 vcc, vcc, s[82:83]\n"
    "s_cmp_eq_u64 vcc, -1\n"
    "s_cbranch_scc0 L_spin_kv_%=\n"
    "L_ok_kv_%=:\n"
    "buffer_load_dwordx4 v[100:103], v1, s[28:31], s41 offen offset:1024 sc1\n"
    "buffer_load_dwordx4 v[160:163], v1, s[28:31], s41 offen offset:3072 sc1\n"
    "v_sub_f32 v209, v37, v209\n"
    "v_sub_f32 v210, v37, v210\n"
    "v_sub_f32 v211, v37, v211\n"
    "v_sub_f32 v212, v37, v212\n"
    "v_sub_f32 v213, v37, v213\n"
    "v_sub_f32 v214, v37, v214\n"
    "v_sub_f32 v215, v37, v215\n"
    "v_sub_f32 v216, v37, v216\n"
    "v_sub_f32 v217, v37, v217\n"
    "v_sub_f32 v218, v37, v218\n"
    "buffer_load_dwordx4 v[96:99], v1, s[28:31], s41 offen sc1\n"
    "v_sub_f32 v219, v37, v219\n"
    "v_sub_f32 v220, v37, v220\n"
    "v_sub_f32 v221, v37, v221\n"
    "v_sub_f32 v222, v37, v222\n"
    "v_sub_f32 v223, v37, v223\n"
    "buffer_load_dwordx4 v[152:155], v1, s[28:31], s41 offen offset:2048 sc1\n"
    "buffer_load_dwordx4 v[92:95], v1, s[28:31], s42 offen offset:1024 sc1\n"
    "buffer_load_dwordx4 v[144:147], v1, s[28:31], s42 offen offset:3072 sc1\n"
    "buffer_load_dwordx4 v[88:91], v1, s[28:31], s42 offen sc1\n"
    "buffer_load_dwordx4 v[136:139], v1, s[28:31], s42 offen offset:2048 sc1\n"
    "s_waitcnt vmcnt(7)\n"
    "s_mov_b32 s57, 0x0\n"
    "s_add_u32 m0, s85, 0x18000\n"
    "v_mfma_f32_32x32x16_bf16 v[184:199], v[100:103], v[64:67], 0\n"
    "buffer_load_dwordx4 v3, s[16:19], s57 offen lds\n"
    "buffer_load_dwordx4 v[84:87], v1, s[28:31], s43 offen offset:1024 sc1\n"
    "buffer_load_dwordx4 v[128:131], v1, s[28:31], s43 offen offset:3072 sc1\n"
    "s_waitcnt vmcnt(8)\n"
    "v_mfma_f32_32x32x16_bf16 v[168:183], v[96:99], v[64:67], 0\n"
    "s_nop 6\n"
    "v_exp_f32 v184, v184\n"
    "v_exp_f32 v185, v185\n"
    "v_exp_f32 v186, v186\n"
    "v_mfma_f32_32x32x16_bf16 v[240:255], v[52:55], v[204:207], v[240:255]\n"
    "v_exp_f32 v187, v187\n"
    "v_exp_f32 v188, v188\n"
    "v_exp_f32 v189, v189\n"
    "buffer_load_dwordx4 v4, s[16:19], s57 offen offset:1024 lds\n"
    "v_permlane16_swap_b32 v160, v164\n"
    "v_permlane16_swap_b32 v161, v165\n"
    "v_permlane16_swap_b32 v162, v166\n"
    "v_permlane16_swap_b32 v163, v167\n"
    "v_exp_f32 v190, v190\n"
    "v_exp_f32 v191, v191\n"
    "v_exp_f32 v192, v192\n"
    "v_cvt_pk_bf16_f32 v200, v184, v185\n"
    "buffer_load_dwordx4 v[80:83], v1, s[28:31], s43 offen sc1\n"
    "v_cvt_pk_bf16_f32 v201, v186, v187\n"
    "v_cvt_pk_bf16_f32 v202, v188, v189\n"
    "v_cvt_pk_bf16_f32 v203, v190, v191\n"
    "v_exp_f32 v193, v193\n"
    "v_exp_f32 v194, v194\n"
    "s_mov_b32 s57, 0x10000\n"
    "s_add_u32 m0, s85, 0x1a000\n"
    "v_mfma_f32_32x32x16_bf16 v[224:239], v[160:163], v[200:203], v[224:239]\n"
    "buffer_load_dwordx4 v3, s[16:19], s57 offen lds\n"
    "v_exp_f32 v195, v195\n"
    "v_exp_f32 v196, v196\n"
    "v_exp_f32 v197, v197\n"
    "v_exp_f32 v198, v198\n"
    "v_exp_f32 v199, v199\n"
    "v_cvt_pk_bf16_f32 v204, v192, v193\n"
    "v_cvt_pk_bf16_f32 v205, v194, v195\n"
    "v_cvt_pk_bf16_f32 v206, v196, v197\n"
    "v_cvt_pk_bf16_f32 v207, v198, v199\n"
    "buffer_load_dwordx4 v4, s[16:19], s57 offen offset:1024 lds\n"
    "s_waitcnt vmcnt(10)\n"
    "v_mfma_f32_32x32x16_bf16 v[184:199], v[92:95], v[64:67], 0\n"
    "v_exp_f32 v168, v168\n"
    "v_exp_f32 v169, v169\n"
    "v_exp_f32 v170, v170\n"
    "v_mfma_f32_32x32x16_bf16 v[224:239], v[164:167], v[204:207], v[224:239]\n"
    "v_exp_f32 v171, v171\n"
    "v_exp_f32 v172, v172\n"
    "s_mov_b32 s57, 0x20000\n"
    "s_add_u32 m0, s85, 0x1c000\n"
    "v_exp_f32 v173, v173\n"
    "buffer_load_dwordx4 v3, s[16:19], s57 offen lds\n"
    "v_permlane16_swap_b32 v152, v156\n"
    "v_permlane16_swap_b32 v153, v157\n"
    "v_permlane16_swap_b32 v154, v158\n"
    "v_permlane16_swap_b32 v155, v159\n"
    "v_exp_f32 v174, v174\n"
    "v_exp_f32 v175, v175\n"
    "buffer_load_dwordx4 v[120:123], v1, s[28:31], s43 offen offset:2048 sc1\n"
    "v_exp_f32 v176, v176\n"
    "v_cvt_pk_bf16_f32 v200, v168, v169\n"
    "v_cvt_pk_bf16_f32 v201, v170, v171\n"
    "v_cvt_pk_bf16_f32 v202, v172, v173\n"
    "v_cvt_pk_bf16_f32 v203, v174, v175\n"
    "v_exp_f32 v177, v177\n"
    "v_exp_f32 v178, v178\n"
    "v_mfma_f32_32x32x16_bf16 v[224:239], v[152:155], v[200:203], v[224:239]\n"
    "buffer_load_dwordx4 v4, s[16:19], s57 offen offset:1024 lds\n"
    "v_exp_f32 v179, v179\n"
    "v_exp_f32 v180, v180\n"
    "v_exp_f32 v181, v181\n"
    "v_exp_f32 v182, v182\n"
    "v_exp_f32 v183, v183\n"
    "v_cvt_pk_bf16_f32 v204, v176, v177\n"
    "v_cvt_pk_bf16_f32 v205, v178, v179\n"
    "v_cvt_pk_bf16_f32 v206, v180, v181\n"
    "s_mov_b32 s57, 0x30000\n"
    "s_add_u32 m0, s85, 0x1e000\n"
    "v_cvt_pk_bf16_f32 v207, v182, v183\n"
    "buffer_load_dwordx4 v3, s[16:19], s57 offen lds\n"
    "s_waitcnt vmcnt(11)\n"
    "v_mfma_f32_32x32x16_bf16 v[168:183], v[88:91], v[64:67], 0\n"
    "v_exp_f32 v184, v184\n"
    "v_exp_f32 v185, v185\n"
    "v_exp_f32 v186, v186\n"
    "v_mfma_f32_32x32x16_bf16 v[224:239], v[156:159], v[204:207], v[224:239]\n"
    "v_exp_f32 v187, v187\n"
    "v_exp_f32 v188, v188\n"
    "v_exp_f32 v189, v189\n"
    "buffer_load_dwordx4 v4, s[16:19], s57 offen offset:1024 lds\n"
    "v_permlane16_swap_b32 v144, v148\n"
    "v_permlane16_swap_b32 v145, v149\n"
    "v_permlane16_swap_b32 v146, v150\n"
    "v_permlane16_swap_b32 v147, v151\n"
    "v_exp_f32 v190, v190\n"
    "v_exp_f32 v191, v191\n"
    "v_exp_f32 v192, v192\n"
    "v_cvt_pk_bf16_f32 v200, v184, v185\n"
    "v_cvt_pk_bf16_f32 v201, v186, v187\n"
    "v_cvt_pk_bf16_f32 v202, v188, v189\n"
    "v_cvt_pk_bf16_f32 v203, v190, v191\n"
    "buffer_load_dwordx4 v[76:79], v1, s[28:31], s44 offen offset:1024 sc1\n"
    "v_exp_f32 v193, v193\n"
    "v_exp_f32 v194, v194\n"
    "s_mov_b32 s57, 0x40000\n"
    "s_add_u32 m0, s85, 0x20000\n"
    "v_mfma_f32_32x32x16_bf16 v[224:239], v[144:147], v[200:203], v[224:239]\n"
    "buffer_load_dwordx4 v3, s[16:19], s57 offen lds\n"
    "v_exp_f32 v195, v195\n"
    "v_exp_f32 v196, v196\n"
    "v_exp_f32 v197, v197\n"
    "v_exp_f32 v198, v198\n"
    "v_exp_f32 v199, v199\n"
    "v_cvt_pk_bf16_f32 v204, v192, v193\n"
    "v_cvt_pk_bf16_f32 v205, v194, v195\n"
    "v_cvt_pk_bf16_f32 v206, v196, v197\n"
    "v_cvt_pk_bf16_f32 v207, v198, v199\n"
    "buffer_load_dwordx4 v4, s[16:19], s57 offen offset:1024 lds\n"
    "s_waitcnt vmcnt(11)\n"
    "v_mfma_f32_32x32x16_bf16 v[184:199], v[84:87], v[64:67], 0\n"
    "v_exp_f32 v168, v168\n"
    "v_exp_f32 v169, v169\n"
    "v_exp_f32 v170, v170\n"
    "v_mfma_f32_32x32x16_bf16 v[224:239], v[148:151], v[204:207], v[224:239]\n"
    "v_exp_f32 v171, v171\n"
    "v_exp_f32 v172, v172\n"
    "v_exp_f32 v173, v173\n"
    "buffer_load_dwordx4 v[112:115], v1, s[28:31], s44 offen offset:3072 sc1\n"
    "s_mov_b32 s57, 0x50000\n"
    "s_add_u32 m0, s85, 0x22000\n"
    "s_nop 0\n"
    "buffer_load_dwordx4 v3, s[16:19], s57 offen lds\n"
    "v_permlane16_swap_b32 v136, v140\n"
    "v_permlane16_swap_b32 v137, v141\n"
    "v_permlane16_swap_b32 v138, v142\n"
    "v_permlane16_swap_b32 v139, v143\n"
    "v_exp_f32 v174, v174\n"
    "v_exp_f32 v175, v175\n"
    "buffer_load_dwordx4 v[72:75], v1, s[28:31], s44 offen sc1\n"
    "v_exp_f32 v176, v176\n"
    "v_cvt_pk_bf16_f32 v200, v168, v169\n"
    "v_cvt_pk_bf16_f32 v201, v170, v171\n"
    "v_cvt_pk_bf16_f32 v202, v172, v173\n"
    "v_cvt_pk_bf16_f32 v203, v174, v175\n"
    "v_exp_f32 v177, v177\n"
    "v_exp_f32 v178, v178\n"
    "v_mfma_f32_32x32x16_bf16 v[224:239], v[136:139], v[200:203], v[224:239]\n"
    "buffer_load_dwordx4 v4, s[16:19], s57 offen offset:1024 lds\n"
    "v_exp_f32 v179, v179\n"
    "v_exp_f32 v180, v180\n"
    "v_exp_f32 v181, v181\n"
    "v_exp_f32 v182, v182\n"
    "v_exp_f32 v183, v183\n"
    "v_cvt_pk_bf16_f32 v204, v176, v177\n"
    "v_cvt_pk_bf16_f32 v205, v178, v179\n"
    "v_cvt_pk_bf16_f32 v206, v180, v181\n"
    "s_mov_b32 s57, 0x60000\n"
    "s_add_u32 m0, s85, 0x24000\n"
    "v_cvt_pk_bf16_f32 v207, v182, v183\n"
    "buffer_load_dwordx4 v3, s[16:19], s57 offen lds\n"
    "s_waitcnt vmcnt(11)\n"
    "v_mfma_f32_32x32x16_bf16 v[168:183], v[80:83], v[64:67], 0\n"
    "v_exp_f32 v184, v184\n"
    "v_exp_f32 v185, v185\n"
    "v_exp_f32 v186, v186\n"
    "v_mfma_f32_32x32x16_bf16 v[224:239], v[140:143], v[204:207], v[224:239]\n"
    "v_exp_f32 v187, v187\n"
    "v_exp_f32 v188, v188\n"
    "v_exp_f32 v189, v189\n"
    "buffer_load_dwordx4 v4, s[16:19], s57 offen offset:1024 lds\n"
    "v_permlane16_swap_b32 v128, v132\n"
    "v_permlane16_swap_b32 v129, v133\n"
    "v_permlane16_swap_b32 v130, v134\n"
    "v_permlane16_swap_b32 v131, v135\n"
    "v_exp_f32 v190, v190\n"
    "v_exp_f32 v191, v191\n"
    "v_exp_f32 v192, v192\n"
    "v_cvt_pk_bf16_f32 v200, v184, v185\n"
    "v_cvt_pk_bf16_f32 v201, v186, v187\n"
    "v_cvt_pk_bf16_f32 v202, v188, v189\n"
    "v_cvt_pk_bf16_f32 v203, v190, v191\n"
    "v_exp_f32 v193, v193\n"
    "v_exp_f32 v194, v194\n"
    "s_mov_b32 s57, 0x70000\n"
    "s_add_u32 m0, s85, 0x26000\n"
    "v_mfma_f32_32x32x16_bf16 v[224:239], v[128:131], v[200:203], v[224:239]\n"
    "buffer_load_dwordx4 v3, s[16:19], s57 offen lds\n"
    "buffer_load_dwordx4 v[104:107], v1, s[28:31], s44 offen offset:2048 sc1\n"
    "v_exp_f32 v195, v195\n"
    "v_exp_f32 v196, v196\n"
    "v_exp_f32 v197, v197\n"
    "v_exp_f32 v198, v198\n"
    "v_exp_f32 v199, v199\n"
    "v_cvt_pk_bf16_f32 v204, v192, v193\n"
    "v_cvt_pk_bf16_f32 v205, v194, v195\n"
    "v_cvt_pk_bf16_f32 v206, v196, v197\n"
    "v_cvt_pk_bf16_f32 v207, v198, v199\n"
    "buffer_load_dwordx4 v4, s[16:19], s57 offen offset:1024 lds\n"
    "s_waitcnt vmcnt(11)\n"
    "v_mfma_f32_32x32x16_bf16 v[184:199], v[76:79], v[64:67], 0\n"
    "v_exp_f32 v168, v168\n"
    "v_exp_f32 v169, v169\n"
    "v_exp_f32 v170, v170\n"
    "v_mfma_f32_32x32x16_bf16 v[224:239], v[132:135], v[204:207], v[224:239]\n"
    "v_exp_f32 v171, v171\n"
    "v_exp_f32 v172, v172\n"
    "v_exp_f32 v173, v173\n"
    "s_mov_b32 s57, 0x0\n"
    "s_add_u32 m0, s45, 0x0\n"
    "v_add_u32 v3, 0x1c00, v6\n"
    "buffer_load_dwordx4 v5, s[20:23], s57 offen lds\n"
    "v_permlane16_swap_b32 v120, v124\n"
    "v_permlane16_swap_b32 v121, v125\n"
    "v_permlane16_swap_b32 v122, v126\n"
    "v_permlane16_swap_b32 v123, v127\n"
    "v_exp_f32 v174, v174\n"
    "v_exp_f32 v175, v175\n"
    "v_exp_f32 v176, v176\n"
    "v_cvt_pk_bf16_f32 v200, v168, v169\n"
    "v_cvt_pk_bf16_f32 v201, v170, v171\n"
    "v_cvt_pk_bf16_f32 v202, v172, v173\n"
    "v_cvt_pk_bf16_f32 v203, v174, v175\n"
    "v_exp_f32 v177, v177\n"
    "v_exp_f32 v178, v178\n"
    "v_mfma_f32_32x32x16_bf16 v[224:239], v[120:123], v[200:203], v[224:239]\n"
    "buffer_load_dwordx4 v17, s[20:23], s57 offen offset:1024 lds\n"
    "v_exp_f32 v179, v179\n"
    "v_exp_f32 v180, v180\n"
    "v_exp_f32 v181, v181\n"
    "v_exp_f32 v182, v182\n"
    "v_exp_f32 v183, v183\n"
    "v_cvt_pk_bf16_f32 v204, v176, v177\n"
    "v_cvt_pk_bf16_f32 v205, v178, v179\n"
    "v_cvt_pk_bf16_f32 v206, v180, v181\n"
    "v_cvt_pk_bf16_f32 v207, v182, v183\n"
    "buffer_load_dwordx4 v24, s[20:23], s57 offen offset:2048 lds\n"
    "s_waitcnt vmcnt(9)\n"
    "v_mfma_f32_32x32x16_bf16 v[168:183], v[72:75], v[64:67], v[208:223]\n"
    "v_exp_f32 v184, v184\n"
    "v_exp_f32 v185, v185\n"
    "v_exp_f32 v186, v186\n"
    "v_mfma_f32_32x32x16_bf16 v[224:239], v[124:127], v[204:207], v[224:239]\n"
    "v_exp_f32 v187, v187\n"
    "v_exp_f32 v188, v188\n"
    "s_mov_b32 s57, 0x15800\n"
    "s_add_u32 m0, s45, 0x3000\n"
    "v_exp_f32 v189, v189\n"
    "buffer_load_dwordx4 v5, s[20:23], s57 offen lds\n"
    "v_permlane16_swap_b32 v112, v116\n"
    "v_permlane16_swap_b32 v113, v117\n"
    "v_permlane16_swap_b32 v114, v118\n"
    "v_permlane16_swap_b32 v115, v119\n"
    "v_exp_f32 v190, v190\n"
    "v_exp_f32 v191, v191\n"
    "v_exp_f32 v192, v192\n"
    "v_cvt_pk_bf16_f32 v200, v184, v185\n"
    "v_cvt_pk_bf16_f32 v201, v186, v187\n"
    "v_cvt_pk_bf16_f32 v202, v188, v189\n"
    "v_cvt_pk_bf16_f32 v203, v190, v191\n"
    "v_exp_f32 v193, v193\n"
    "v_exp_f32 v194, v194\n"
    "v_mfma_f32_32x32x16_bf16 v[224:239], v[112:115], v[200:203], v[224:239]\n"
    "buffer_load_dwordx4 v17, s[20:23], s57 offen offset:1024 lds\n"
    "v_exp_f32 v195, v195\n"
    "v_exp_f32 v196, v196\n"
    "v_exp_f32 v197, v197\n"
    "v_exp_f32 v198, v198\n"
    "v_exp_f32 v199, v199\n"
    "v_cvt_pk_bf16_f32 v204, v192, v193\n"
    "v_cvt_pk_bf16_f32 v205, v194, v195\n"
    "v_cvt_pk_bf16_f32 v206, v196, v197\n"
    "v_cvt_pk_bf16_f32 v207, v198, v199\n"
    "buffer_load_dwordx4 v24, s[20:23], s57 offen offset:2048 lds\n"
    "v_mfma_f32_32x32x16_bf16 v[184:199], v[100:103], v[68:71], 0\n"
    "v_exp_f32 v168, v168\n"
    "v_exp_f32 v169, v169\n"
    "v_exp_f32 v170, v170\n"
    "v_mfma_f32_32x32x16_bf16 v[224:239], v[116:119], v[204:207], v[224:239]\n"
    "v_exp_f32 v171, v171\n"
    "v_exp_f32 v172, v172\n"
    "s_mov_b32 s57, 0xac000\n"
    "s_add_u32 m0, s45, 0x6000\n"
    "v_exp_f32 v173, v173\n"
    "buffer_load_dwordx4 v5, s[20:23], s57 offen lds\n"
    "s_waitcnt vmcnt(8)\n"
    "v_permlane16_swap_b32 v104, v108\n"
    "v_permlane16_swap_b32 v105, v109\n"
    "v_permlane16_swap_b32 v106, v110\n"
    "v_permlane16_swap_b32 v107, v111\n"
    "v_exp_f32 v174, v174\n"
    "v_exp_f32 v175, v175\n"
    "v_exp_f32 v176, v176\n"
    "v_cvt_pk_bf16_f32 v200, v168, v169\n"
    "v_cvt_pk_bf16_f32 v201, v170, v171\n"
    "v_cvt_pk_bf16_f32 v202, v172, v173\n"
    "v_cvt_pk_bf16_f32 v203, v174, v175\n"
    "v_exp_f32 v177, v177\n"
    "v_exp_f32 v178, v178\n"
    "v_mfma_f32_32x32x16_bf16 v[224:239], v[104:107], v[200:203], v[224:239]\n"
    "buffer_load_dwordx4 v17, s[20:23], s57 offen offset:1024 lds\n"
    "v_exp_f32 v179, v179\n"
    "v_exp_f32 v180, v180\n"
    "v_exp_f32 v181, v181\n"
    "v_exp_f32 v182, v182\n"
    "v_exp_f32 v183, v183\n"
    "v_cvt_pk_bf16_f32 v204, v176, v177\n"
    "v_cvt_pk_bf16_f32 v205, v178, v179\n"
    "v_cvt_pk_bf16_f32 v206, v180, v181\n"
    "v_cvt_pk_bf16_f32 v207, v182, v183\n"
    "buffer_load_dwordx4 v24, s[20:23], s57 offen offset:2048 lds\n"
    "v_mfma_f32_32x32x16_bf16 v[168:183], v[96:99], v[68:71], 0\n"
    "v_exp_f32 v184, v184\n"
    "v_exp_f32 v185, v185\n"
    "v_exp_f32 v186, v186\n"
    "v_mfma_f32_32x32x16_bf16 v[224:239], v[108:111], v[204:207], v[224:239]\n"
    "v_exp_f32 v187, v187\n"
    "v_exp_f32 v188, v188\n"
    "s_mov_b32 s57, 0xc1800\n"
    "s_add_u32 m0, s45, 0x9000\n"
    "v_exp_f32 v189, v189\n"
    "buffer_load_dwordx4 v5, s[20:23], s57 offen lds\n"
    "v_exp_f32 v190, v190\n"
    "s_nop 4\n"
    "v_cmp_le_f32 vcc, 0x1f800000, v232\n"
    "s_mov_b32 s48, 0x5f800000\n"
    "v_cmp_ge_f32 s[48:49], s48, v232\n"
    "v_rcp_f32 v234, v232\n"
    "s_and_b64 vcc, s[48:49], vcc\n"
    "s_cmp_eq_u64 vcc, -1\n"
    "s_cbranch_scc0 L_slowa_%=\n"
    "L_att_donea_%=:\n"
    "v_exp_f32 v191, v191\n"
    "v_mul_f32 v224, v224, v234\n"
    "v_mul_f32 v225, v225, v234\n"
    "v_mul_f32 v226, v226, v234\n"
    "v_mul_f32 v227, v227, v234\n"
    "v_mul_f32 v228, v228, v234\n"
    "v_mul_f32 v229, v229, v234\n"
    "v_mul_f32 v230, v230, v234\n"
    "v_mul_f32 v231, v231, v234\n"
    "v_cvt_pk_bf16_f32 v236, v224, v225\n"
    "v_cvt_pk_bf16_f32 v237, v226, v227\n"
    "v_cvt_pk_bf16_f32 v238, v228, v229\n"
    "v_cvt_pk_bf16_f32 v239, v230, v231\n"
    "v_add_u32 v4, 0x3800, v6\n"
    "buffer_store_dwordx4 v[236:239], v2, s[28:31], s92 offen sc1\n"
    "v_exp_f32 v192, v192\n"
    "v_cvt_pk_bf16_f32 v200, v184, v185\n"
    "v_cvt_pk_bf16_f32 v201, v186, v187\n"
    "v_cvt_pk_bf16_f32 v202, v188, v189\n"
    "v_cvt_pk_bf16_f32 v203, v190, v191\n"
    "v_exp_f32 v193, v193\n"
    "v_exp_f32 v194, v194\n"
    "v_mfma_f32_32x32x16_bf16 v[240:255], v[160:163], v[200:203], v[240:255]\n"
    "buffer_load_dwordx4 v17, s[20:23], s57 offen offset:1024 lds\n"
    "v_exp_f32 v195, v195\n"
    "v_exp_f32 v196, v196\n"
    "v_exp_f32 v197, v197\n"
    "v_exp_f32 v198, v198\n"
    "v_exp_f32 v199, v199\n"
    "v_cvt_pk_bf16_f32 v204, v192, v193\n"
    "v_cvt_pk_bf16_f32 v205, v194, v195\n"
    "v_cvt_pk_bf16_f32 v206, v196, v197\n"
    "v_cvt_pk_bf16_f32 v207, v198, v199\n"
    "buffer_load_dwordx4 v24, s[20:23], s57 offen offset:2048 lds\n"
    "v_mfma_f32_32x32x16_bf16 v[184:199], v[92:95], v[68:71], 0\n"
    "v_exp_f32 v168, v168\n"
    "v_exp_f32 v169, v169\n"
    "v_exp_f32 v170, v170\n"
    "v_mfma_f32_32x32x16_bf16 v[240:255], v[164:167], v[204:207], v[240:255]\n"
    "v_exp_f32 v171, v171\n"
    "v_exp_f32 v172, v172\n"
    "s_mov_b32 s57, 0x2b000\n"
    "s_add_u32 m0, s45, 0xc000\n"
    "v_exp_f32 v173, v173\n"
    "buffer_load_dwordx4 v5, s[20:23], s57 offen lds\n"
    "v_exp_f32 v174, v174\n"
    "v_exp_f32 v175, v175\n"
    "v_exp_f32 v176, v176\n"
    "v_cvt_pk_bf16_f32 v200, v168, v169\n"
    "v_cvt_pk_bf16_f32 v201, v170, v171\n"
    "v_cvt_pk_bf16_f32 v202, v172, v173\n"
    "v_cvt_pk_bf16_f32 v203, v174, v175\n"
    "v_exp_f32 v177, v177\n"
    "v_exp_f32 v178, v178\n"
    "v_mfma_f32_32x32x16_bf16 v[240:255], v[152:155], v[200:203], v[240:255]\n"
    "buffer_load_dwordx4 v17, s[20:23], s57 offen offset:1024 lds\n"
    "v_exp_f32 v179, v179\n"
    "v_exp_f32 v180, v180\n"
    "v_exp_f32 v181, v181\n"
    "v_exp_f32 v182, v182\n"
    "v_exp_f32 v183, v183\n"
    "v_cvt_pk_bf16_f32 v204, v176, v177\n"
    "v_cvt_pk_bf16_f32 v205, v178, v179\n"
    "v_cvt_pk_bf16_f32 v206, v180, v181\n"
    "v_cvt_pk_bf16_f32 v207, v182, v183\n"
    "buffer_load_dwordx4 v24, s[20:23], s57 offen offset:2048 lds\n"
    "v_mfma_f32_32x32x16_bf16 v[168:183], v[88:91], v[68:71], 0\n"
    "v_exp_f32 v184, v184\n"
    "v_exp_f32 v185, v185\n"
    "v_exp_f32 v186, v186\n"
    "v_mfma_f32_32x32x16_bf16 v[240:255], v[156:159], v[204:207], v[240:255]\n"
    "v_exp_f32 v187, v187\n"
    "v_exp_f32 v188, v188\n"
    "s_mov_b32 s57, 0x40800\n"
    "s_add_u32 m0, s45, 0xf000\n"
    "v_exp_f32 v189, v189\n"
    "buffer_load_dwordx4 v5, s[20:23], s57 offen lds\n"
    "v_exp_f32 v190, v190\n"
    "v_exp_f32 v191, v191\n"
    "v_exp_f32 v192, v192\n"
    "v_cvt_pk_bf16_f32 v200, v184, v185\n"
    "v_cvt_pk_bf16_f32 v201, v186, v187\n"
    "v_cvt_pk_bf16_f32 v202, v188, v189\n"
    "v_cvt_pk_bf16_f32 v203, v190, v191\n"
    "v_exp_f32 v193, v193\n"
    "v_exp_f32 v194, v194\n"
    "v_mfma_f32_32x32x16_bf16 v[240:255], v[144:147], v[200:203], v[240:255]\n"
    "buffer_load_dwordx4 v17, s[20:23], s57 offen offset:1024 lds\n"
    "v_exp_f32 v195, v195\n"
    "v_exp_f32 v196, v196\n"
    "v_exp_f32 v197, v197\n"
    "v_exp_f32 v198, v198\n"
    "v_exp_f32 v199, v199\n"
    "v_cvt_pk_bf16_f32 v204, v192, v193\n"
    "v_cvt_pk_bf16_f32 v205, v194, v195\n"
    "v_cvt_pk_bf16_f32 v206, v196, v197\n"
    "v_cvt_pk_bf16_f32 v207, v198, v199\n"
    "s_waitcnt vmcnt(7)\n"
    "buffer_store_dword v27, v31, s[28:31], s3 offen offset:128 sc1\n"
    "v_mov_b32 v28, 0\n"
    "s_add_u32 s92, s93, 0x60000\n"
    "buffer_load_dwordx4 v24, s[20:23], s57 offen offset:2048 lds\n"
    "v_mfma_f32_32x32x16_bf16 v[184:199], v[84:87], v[68:71], 0\n"
    "v_exp_f32 v168, v168\n"
    "v_exp_f32 v169, v169\n"
    "v_exp_f32 v170, v170\n"
    "v_mfma_f32_32x32x16_bf16 v[240:255], v[148:151], v[204:207], v[240:255]\n"
    "v_exp_f32 v171, v171\n"
    "v_exp_f32 v172, v172\n"
    "s_mov_b32 s57, 0xd7000\n"
    "s_add_u32 m0, s45, 0x12000\n"
    "v_exp_f32 v173, v173\n"
    "buffer_load_dwordx4 v5, s[20:23], s57 offen lds\n"
    "v_exp_f32 v174, v174\n"
    "v_exp_f32 v175, v175\n"
    "v_exp_f32 v176, v176\n"
    "v_cvt_pk_bf16_f32 v200, v168, v169\n"
    "v_cvt_pk_bf16_f32 v201, v170, v171\n"
    "v_cvt_pk_bf16_f32 v202, v172, v173\n"
    "v_cvt_pk_bf16_f32 v203, v174, v175\n"
    "v_exp_f32 v177, v177\n"
    "v_exp_f32 v178, v178\n"
    "v_mfma_f32_32x32x16_bf16 v[240:255], v[136:139], v[200:203], v[240:255]\n"
    "buffer_load_dwordx4 v17, s[20:23], s57 offen offset:1024 lds\n"
    "v_exp_f32 v179, v179\n"
    "v_exp_f32 v180, v180\n"
    "v_exp_f32 v181, v181\n"
    "v_exp_f32 v182, v182\n"
    "v_exp_f32 v183, v183\n"
    "v_cvt_pk_bf16_f32 v204, v176, v177\n"
    "v_cvt_pk_bf16_f32 v205, v178, v179\n"
    "v_cvt_pk_bf16_f32 v206, v180, v181\n"
    "v_cvt_pk_bf16_f32 v207, v182, v183\n"
    "buffer_load_dwordx4 v24, s[20:23], s57 offen offset:2048 lds\n"
    "v_mfma_f32_32x32x16_bf16 v[168:183], v[80:83], v[68:71], 0\n"
    "v_exp_f32 v184, v184\n"
    "v_exp_f32 v185, v185\n"
    "v_exp_f32 v186, v186\n"
    "v_mfma_f32_32x32x16_bf16 v[240:255], v[140:143], v[204:207], v[240:255]\n"
    "v_exp_f32 v187, v187\n"
    "v_exp_f32 v188, v188\n"
    "s_mov_b32 s57, 0xec800\n"
    "s_add_u32 m0, s45, 0x15000\n"
    "v_exp_f32 v189, v189\n"
    "buffer_load_dwordx4 v5, s[20:23], s57 offen lds\n"
    "v_exp_f32 v190, v190\n"
    "v_exp_f32 v191, v191\n"
    "v_exp_f32 v192, v192\n"
    "v_cvt_pk_bf16_f32 v200, v184, v185\n"
    "v_cvt_pk_bf16_f32 v201, v186, v187\n"
    "v_cvt_pk_bf16_f32 v202, v188, v189\n"
    "v_cvt_pk_bf16_f32 v203, v190, v191\n"
    "v_exp_f32 v193, v193\n"
    "v_exp_f32 v194, v194\n"
    "v_mfma_f32_32x32x16_bf16 v[240:255], v[128:131], v[200:203], v[240:255]\n"
    "buffer_load_dwordx4 v17, s[20:23], s57 offen offset:1024 lds\n"
    "v_exp_f32 v195, v195\n"
    "v_exp_f32 v196, v196\n"
    "v_exp_f32 v197, v197\n"
    "v_exp_f32 v198, v198\n"
    "v_exp_f32 v199, v199\n"
    "v_cvt_pk_bf16_f32 v204, v192, v193\n"
    "v_cvt_pk_bf16_f32 v205, v194, v195\n"
    "v_cvt_pk_bf16_f32 v206, v196, v197\n"
    "v_cvt_pk_bf16_f32 v207, v198, v199\n"
    "buffer_load_dwordx4 v24, s[20:23], s57 offen offset:2048 lds\n"
    "v_mfma_f32_32x32x16_bf16 v[184:199], v[76:79], v[68:71], v[208:223]\n"
    "v_exp_f32 v168, v168\n"
    "v_exp_f32 v169, v169\n"
    "v_exp_f32 v170, v170\n"
    "v_mfma_f32_32x32x16_bf16 v[240:255], v[132:135], v[204:207], v[240:255]\n"
    "v_exp_f32 v171, v171\n"
    "v_exp_f32 v172, v172\n"
    "v_exp_f32 v173, v173\n"
    "v_exp_f32 v174, v174\n"
    "v_exp_f32 v175, v175\n"
    "v_exp_f32 v176, v176\n"
    "v_cvt_pk_bf16_f32 v200, v168, v169\n"
    "v_cvt_pk_bf16_f32 v201, v170, v171\n"
    "v_cvt_pk_bf16_f32 v202, v172, v173\n"
    "v_cvt_pk_bf16_f32 v203, v174, v175\n"
    "v_exp_f32 v177, v177\n"
    "v_exp_f32 v178, v178\n"
    "v_mfma_f32_32x32x16_bf16 v[240:255], v[120:123], v[200:203], v[240:255]\n"
    "v_exp_f32 v179, v179\n"
    "v_exp_f32 v180, v180\n"
    "v_exp_f32 v181, v181\n"
    "v_exp_f32 v182, v182\n"
    "v_exp_f32 v183, v183\n"
    "v_cvt_pk_bf16_f32 v204, v176, v177\n"
    "v_cvt_pk_bf16_f32 v205, v178, v179\n"
    "v_cvt_pk_bf16_f32 v206, v180, v181\n"
    "v_cvt_pk_bf16_f32 v207, v182, v183\n"
    "v_exp_f32 v184, v184\n"
    "v_exp_f32 v185, v185\n"
    "v_exp_f32 v186, v186\n"
    "v_mfma_f32_32x32x16_bf16 v[240:255], v[124:127], v[204:207], v[240:255]\n"
    "v_exp_f32 v187, v187\n"
    "v_exp_f32 v188, v188\n"
    "v_exp_f32 v189, v189\n"
    "v_exp_f32 v190, v190\n"
    "v_exp_f32 v191, v191\n"
    "v_exp_f32 v192, v192\n"
    "v_cvt_pk_bf16_f32 v200, v184, v185\n"
    "v_cvt_pk_bf16_f32 v201, v186, v187\n"
    "v_cvt_pk_bf16_f32 v202, v188, v189\n"
    "v_cvt_pk_bf16_f32 v203, v190, v191\n"
    "v_exp_f32 v193, v193\n"
    "v_exp_f32 v194, v194\n"
    "v_mfma_f32_32x32x16_bf16 v[240:255], v[112:115], v[200:203], v[240:255]\n"
    "v_exp_f32 v195, v195\n"
    "v_exp_f32 v196, v196\n"
    "v_exp_f32 v197, v197\n"
    "v_exp_f32 v198, v198\n"
    "s_waitcnt vmcnt(7)\n"
    "s_barrier\n"
    "ds_read_b64_tr_b16 a[192:193], v25\n"
    "ds_read_b64_tr_b16 a[194:195], v25 offset:1024\n"
    "ds_read_b64_tr_b16 a[196:197], v25 offset:4096\n"
    "ds_read_b64_tr_b16 a[198:199], v25 offset:5120\n"
    "ds_read_b64_tr_b16 a[200:201], v25 offset:8192\n"
    "v_exp_f32 v199, v199\n"
    "ds_read_b64_tr_b16 a[202:203], v25 offset:9216\n"
    "ds_read_b64_tr_b16 a[204:205], v25 offset:12288\n"
    "ds_read_b64_tr_b16 a[206:207], v25 offset:13312\n"
    "v_cvt_pk_bf16_f32 v204, v192, v193\n"
    "ds_read_b64_tr_b16 a[208:209], v25 offset:16384\n"
    "ds_read_b64_tr_b16 a[210:211], v25 offset:17408\n"
    "v_cvt_pk_bf16_f32 v205, v194, v195\n"
    "ds_read_b64_tr_b16 a[212:213], v25 offset:20480\n"
    "ds_read_b64_tr_b16 a[214:215], v25 offset:21504\n"
    "v_cvt_pk_bf16_f32 v206, v196, v197\n"
    "ds_read_b64_tr_b16 a[216:217], v25 offset:24576\n"
    "ds_read_b64_tr_b16 a[218:219], v25 offset:25600\n"
    "v_cvt_pk_bf16_f32 v207, v198, v199\n"
    "ds_read_b64_tr_b16 a[220:221], v25 offset:28672\n"
    "ds_read_b64_tr_b16 a[222:223], v25 offset:29696\n"
    "v_mfma_f32_32x32x16_bf16 v[240:255], v[116:119], v[204:207], v[240:255]\n"
    "ds_read_b64_tr_b16 a[224:225], v25 offset:32768\n"
    "ds_read_b64_tr_b16 a[226:227], v25 offset:33792\n"
    "ds_read_b64_tr_b16 a[228:229], v25 offset:36864\n"
    "ds_read_b64_tr_b16 a[230:231], v25 offset:37888\n"
    "ds_read_b64_tr_b16 a[232:233], v25 offset:40960\n"
    "ds_read_b64_tr_b16 a[234:235], v25 offset:41984\n"
    "ds_read_b64_tr_b16 a[236:237], v25 offset:45056\n"
    "ds_read_b64_tr_b16 a[238:239], v25 offset:46080\n"
    "ds_read_b64_tr_b16 a[240:241], v25 offset:49152\n"
    "buffer_atomic_or v28, v33, s[28:31], s4 offen offset:128 sc0 sc1\n"
    "ds_read_b64_tr_b16 a[242:243], v25 offset:50176\n"
    "ds_read_b64_tr_b16 a[244:245], v25 offset:53248\n"
    "ds_read_b64_tr_b16 a[246:247], v25 offset:54272\n"
    "ds_read_b64_tr_b16 a[248:249], v25 offset:57344\n"
    "ds_read_b64_tr_b16 a[250:251], v25 offset:58368\n"
    "ds_read_b64_tr_b16 a[252:253], v25 offset:61440\n"
    "ds_read_b64_tr_b16 a[254:255], v25 offset:62464\n"
    "v_cmp_le_f32 vcc, 0x1f800000, v248\n"
    "s_mov_b32 s48, 0x5f800000\n"
    "v_cmp_ge_f32 s[48:49], s48, v248\n"
    "v_rcp_f32 v250, v248\n"
    "s_and_b64 vcc, s[48:49], vcc\n"
    "s_cmp_eq_u64 vcc, -1\n"
    "s_cbranch_scc0 L_slowb_%=\n"
    "L_att_doneb_%=:\n"
    "v_lshrrev_b32 v154, 2, v34\n"
    "v_pk_mul_f32 v[240:241], v[240:241], v[250:251] op_sel_hi:[1,0]\n"
    "v_pk_mul_f32 v[242:243], v[242:243], v[250:251] op_sel_hi:[1,0]\n"
    "v_pk_mul_f32 v[244:245], v[244:245], v[250:251] op_sel_hi:[1,0]\n"
    "v_pk_mul_f32 v[246:247], v[246:247], v[250:251] op_sel_hi:[1,0]\n"
    "v_cvt_pk_bf16_f32 v252, v240, v241\n"
    "v_cvt_pk_bf16_f32 v253, v242, v243\n"
    "v_cvt_pk_bf16_f32 v254, v244, v245\n"
    "v_cvt_pk_bf16_f32 v255, v246, v247\n"
    "v_lshrrev_b32 v155, 4, v34\n"
    "buffer_store_dwordx4 v[252:255], v2, s[28:31], s94 offen sc1\n"
    "v_xor_b32 v154, v154, v155\n"
    "v_and_b32 v155, 0x100, v1\n"
    "v_and_b32 v154, 1, v154\n"
    "v_xor_b32 v136, s54, v18\n"
    "s_waitcnt lgkmcnt(0)\n"
    "s_waitcnt vmcnt(2)\n"
    "s_barrier\n"
    "v_subrev_u32 v154, 1, v154\n"
    "s_mul_i32 s54, s52, 24576\n"
    "v_and_b32 v112, 0x3f803f80, v154\n"
    "v_and_b32 v113, 0x3f803f80, v154\n"
    "v_and_b32 v114, 0x3f803f80, v154\n"
    "v_and_b32 v115, 0x3f803f80, v154\n"
    "v_and_b32 v154, 0xf0, v1\n"
    "s_add_u32 s55, s54, s53\n"
    "v_lshl_add_u32 v154, v155, 1, v154\n"
    "v_lshrrev_b32 v155, 5, v34\n"
    "v_add_u32 v138, s55, v136\n"
    "v_lshl_add_u32 v137, v155, 13, v154\n"
    "ds_read_b64_tr_b16 a[0:1], v138\n"
    "ds_read_b64_tr_b16 a[2:3], v138 offset:3072\n"
    "ds_read_b64_tr_b16 a[4:5], v138 offset:6144\n"
    "ds_read_b64_tr_b16 a[6:7], v138 offset:9216\n"
    "ds_read_b64_tr_b16 a[8:9], v138 offset:12288\n"
    "v_add_u32 v137, 0x18000, v137\n"
    "ds_read_b64_tr_b16 a[10:11], v138 offset:15360\n"
    "ds_read_b64_tr_b16 a[12:13], v138 offset:18432\n"
    "s_sub_u32 s55, s53, s54\n"
    "ds_read_b64_tr_b16 a[14:15], v138 offset:21504\n"
    "s_lshl_b32 s53, s74, 5\n"
    "s_add_u32 s55, s55, 24576\n"
    "v_xor_b32 v140, s53, v18\n"
    "s_add_u32 s54, s54, 2048\n"
    "v_add_u32 v139, s55, v136\n"
    "v_add_u32 v140, s54, v140\n"
    "ds_read_b64_tr_b16 a[64:65], v139\n"
    "ds_read_b64_tr_b16 a[128:129], v140\n"
    "ds_read_b64_tr_b16 a[130:131], v140 offset:3072\n"
    "ds_read_b64_tr_b16 a[66:67], v139 offset:3072\n"
    "ds_read_b64_tr_b16 a[132:133], v140 offset:6144\n"
    "v_lshlrev_b32 v48, 16, v8\n"
    "ds_read_b64_tr_b16 a[134:135], v140 offset:9216\n"
    "ds_read_b64_tr_b16 a[68:69], v139 offset:6144\n"
    "v_and_b32 v49, 0xffff0000, v8\n"
    "ds_read_b64_tr_b16 a[70:71], v139 offset:9216\n"
    "ds_read_b64_tr_b16 a[136:137], v140 offset:12288\n"
    "v_lshlrev_b32 v52, 16, v10\n"
    "ds_read_b64_tr_b16 a[138:139], v140 offset:15360\n"
    "ds_read_b64_tr_b16 a[72:73], v139 offset:12288\n"
    "v_and_b32 v53, 0xffff0000, v10\n"
    "ds_read_b64_tr_b16 a[74:75], v139 offset:15360\n"
    "ds_read_b64_tr_b16 a[140:141], v140 offset:18432\n"
    "v_lshlrev_b32 v50, 16, v9\n"
    "ds_read_b64_tr_b16 a[142:143], v140 offset:21504\n"
    "ds_read_b64_tr_b16 a[76:77], v139 offset:18432\n"
    "v_and_b32 v51, 0xffff0000, v9\n"
    "ds_read_b64_tr_b16 a[78:79], v139 offset:21504\n"
    "v_lshlrev_b32 v54, 16, v11\n"
    "v_and_b32 v55, 0xffff0000, v11\n"
    "s_add_u32 s54, s85, s79\n"
    "v_lshl_add_u32 v143, v155, 8, v154\n"
    "s_lshl_b32 s52, s74, 14\n"
    "s_add_u32 s60, s54, 0xe0000\n"
    "s_waitcnt vmcnt(1)\n"
    "v_cmp_eq_u32 vcc, v28, v27\n"
    "s_nop 0\n"
    "s_or_b64 vcc, vcc, s[98:99]\n"
    "s_cmp_eq_u64 vcc, -1\n"
    "s_cbranch_scc0 L_spin_oa_%=\n"
    "L_ok_oa_%=:\n"
    "s_add_u32 m0, s47, 0x18000\n"
    "s_nop 0\n"
    "buffer_load_dwordx4 v2, s[28:31], s62 offen sc1 lds\n"
    "buffer_load_dwordx4 v2, s[28:31], s62 offen offset:1024 sc1 lds\n"
    "s_lshl_b32 s89, s76, 8\n"
    "v_add_u32 v143, s52, v143\n"
    "v_lshlrev_b32 v150, 2, v34\n"
    "s_add_u32 s89, s89, s60\n"
    "s_add_u32 s61, s54, 0xe4000\n"
    "v_add_u32 v151, 49152, v138\n"
    "v_add_u32 v153, 49152, v140\n"
    "ds_read_b64_tr_b16 a[16:17], v151\n"
    "ds_read_b64_tr_b16 a[18:19], v151 offset:3072\n"
    "buffer_load_dwordx4 v2, s[28:31], s62 offen offset:2048 sc1 lds\n"
    "ds_read_b64_tr_b16 a[144:145], v153\n"
    "ds_read_b64_tr_b16 a[146:147], v153 offset:3072\n"
    "ds_read_b64_tr_b16 a[20:21], v151 offset:6144\n"
    "ds_read_b64_tr_b16 a[22:23], v151 offset:9216\n"
    "ds_read_b64_tr_b16 a[148:149], v153 offset:6144\n"
    "s_waitcnt lgkmcnt(7)\n"
    "s_barrier\n"
    "buffer_load_dwordx4 v2, s[28:31], s62 offen offset:3072 sc1 lds\n"
    "ds_read_b64_tr_b16 a[150:151], v153 offset:9216\n"
    "ds_read_b64_tr_b16 a[24:25], v151 offset:12288\n"
    "ds_read_b64_tr_b16 a[26:27], v151 offset:15360\n"
    "ds_read_b64_tr_b16 a[152:153], v153 offset:12288\n"
    "ds_read_b64_tr_b16 a[154:155], v153 offset:15360\n"
    "v_add_u32 v152, 49152, v139\n"
    "ds_read_b64_tr_b16 a[28:29], v151 offset:18432\n"
    "s_add_u32 m0, s47, 0x19000\n"
    "ds_read_b64_tr_b16 a[80:81], v152\n"
    "buffer_load_dwordx4 v2, s[28:31], s63 offen sc1 lds\n"
    "ds_read_b64_tr_b16 a[82:83], v152 offset:3072\n"
    "ds_read_b64_tr_b16 a[84:85], v152 offset:6144\n"
    "ds_read_b64_tr_b16 a[86:87], v152 offset:9216\n"
    "ds_read_b64_tr_b16 a[88:89], v152 offset:12288\n"
    "s_and_b32 s54, s74, 1\n"
    "ds_read_b64_tr_b16 a[90:91], v152 offset:15360\n"
    "s_lshl_b32 s54, s54, 12\n"
    "ds_read_b64_tr_b16 a[30:31], v151 offset:21504\n"
    "s_add_u32 s54, s54, 0x18000\n"
    "ds_read_b64_tr_b16 a[156:157], v153 offset:18432\n"
    "v_add_u32 v144, s54, v1\n"
    "ds_read_b64_tr_b16 a[158:159], v153 offset:21504\n"
    "buffer_load_dwordx4 v2, s[28:31], s63 offen offset:1024 sc1 lds\n"
    "ds_read_b64_tr_b16 a[92:93], v152 offset:18432\n"
    "ds_read_b64_tr_b16 a[94:95], v152 offset:21504\n"
    "s_min_u32 s52, s76, 3\n"
    "s_mul_i32 s53, s76, 5\n"
    "v_lshrrev_b32 v154, 5, v34\n"
    "s_add_u32 s52, s52, s53\n"
    "v_lshlrev_b32 v155, 1, v34\n"
    "s_add_u32 s53, s52, s74\n"
    "v_lshl_add_u32 v154, v154, 24, v155\n"
    "s_lshl_b32 s53, s53, 6\n"
    "s_lshl_b32 s52, s52, 1\n"
    "buffer_load_dwordx4 v2, s[28:31], s63 offen offset:2048 sc1 lds\n"
    "s_add_u32 s52, s52, s74\n"
    "v_mfma_f32_16x16x32_bf16 v[104:107], v[112:115], a[0:3], 0\n"
    "s_lshl_b32 s52, s52, 5\n"
    "s_add_u32 s54, s85, 0x12000\n"
    "v_mfma_f32_16x16x32_bf16 v[104:107], v[112:115], a[64:67], v[104:107]\n"
    "v_add_u32 v146, s54, v1\n"
    "s_lshl_b32 s54, s74, 8\n"
    "v_mfma_f32_16x16x32_bf16 v[104:107], v[112:115], a[4:7], v[104:107]\n"
    "s_waitcnt lgkmcnt(0)\n"
    "s_barrier\n"
    "s_waitcnt vmcnt(7)\n"
    "buffer_store_dword v27, v31, s[28:31], s3 offen offset:256 sc1\n"
    "v_mov_b32 v16, 0\n"
    "v_mfma_f32_16x16x32_bf16 v[104:107], v[112:115], a[68:71], v[104:107]\n"
    "s_add_u32 s54, s54, 0x14000\n"
    "v_xor_b32 v145, 0x1000, v144\n"
    "v_mfma_f32_16x16x32_bf16 v[104:107], v[112:115], a[8:11], v[104:107]\n"
    "v_mov_b32 v123, 0\n"
    "v_mov_b32 v127, 0\n"
    "v_mfma_f32_16x16x32_bf16 v[104:107], v[112:115], a[72:75], v[104:107]\n"
    "v_mov_b32 v119, 0\n"
    "v_xor_b32 v147, 0x800, v146\n"
    "v_mfma_f32_16x16x32_bf16 v[104:107], v[112:115], a[12:15], v[104:107]\n"
    "buffer_load_dwordx4 v2, s[28:31], s63 offen offset:3072 sc1 lds\n"
    "v_mfma_f32_16x16x32_bf16 v[104:107], v[112:115], a[76:79], v[104:107]\n"
    "s_cmp_ge_u32 s74, 2\n"
    "s_cselect_b32 s91, 0x40000000, 0\n"
    "v_mfma_f32_16x16x32_bf16 v[104:107], v[112:115], a[16:19], v[104:107]\n"
    "v_add_u32 v7, s91, v7\n"
    "v_add_u32 v141, s46, v18\n"
    "v_mfma_f32_16x16x32_bf16 v[104:107], v[112:115], a[80:83], v[104:107]\n"
    "v_add_u32 v32, 0x1c00, v7\n"
    "v_xor_b32 v142, 64, v141\n"
    "v_mfma_f32_16x16x32_bf16 v[104:107], v[112:115], a[20:23], v[104:107]\n"
    "s_mov_b32 s57, 0x56000\n"
    "s_add_u32 m0, s45, 0x0\n"
    "v_add_u32 v39, s91, v1\n"
    "buffer_load_dwordx4 v5, s[20:23], s57 offen lds\n"
    "v_mfma_f32_16x16x32_bf16 v[104:107], v[112:115], a[84:87], v[104:107]\n"
    "v_and_b32 v38, 7, v34\n"
    "v_lshrrev_b32 v37, 5, v34\n"
    "v_mfma_f32_16x16x32_bf16 v[104:107], v[112:115], a[24:27], v[104:107]\n"
    "v_xor_b32 v136, v37, v38\n"
    "v_and_b32 v36, 31, v34\n"
    "v_mfma_f32_16x16x32_bf16 v[104:107], v[112:115], a[88:91], v[104:107]\n"
    "v_lshlrev_b32 v136, 4, v136\n"
    "s_mov_b32 s55, 0x8000\n"
    "v_mfma_f32_16x16x32_bf16 v[104:107], v[112:115], a[28:31], v[104:107]\n"
    "buffer_load_dwordx4 v17, s[20:23], s57 offen offset:1024 lds\n"
    "v_mfma_f32_16x16x32_bf16 v[104:107], v[112:115], a[92:95], v[104:107]\n"
    "v_lshl_add_u32 v136, v36, 8, v136\n"
    "v_mfma_f32_16x16x32_bf16 v[108:111], v[112:115], a[128:131], 0\n"
    "v_mfma_f32_16x16x32_bf16 v[108:111], v[112:115], a[132:135], v[108:111]\n"
    "v_mfma_f32_16x16x32_bf16 v[108:111], v[112:115], a[136:139], v[108:111]\n"
    "buffer_load_dwordx4 v24, s[20:23], s57 offen offset:2048 lds\n"
    "s_waitcnt vmcnt(8)\n"
    "s_barrier\n"
    "ds_read_b128 v[72:75], v137\n"
    "ds_read_b128 v[76:79], v137 offset:256\n"
    "v_mfma_f32_16x16x32_bf16 v[108:111], v[112:115], a[140:143], v[108:111]\n"
    "s_mov_b32 s57, 0x6b800\n"
    "s_add_u32 m0, s45, 0x3000\n"
    "ds_read_b128 v[80:83], v137 offset:16384\n"
    "buffer_load_dwordx4 v5, s[20:23], s57 offen lds\n"
    "ds_read_b128 v[84:87], v137 offset:16640\n"
    "v_mfma_f32_16x16x32_bf16 v[108:111], v[112:115], a[144:147], v[108:111]\n"
    "ds_read_b128 v[88:91], v137 offset:1024\n"
    "v_mfma_f32_16x16x32_bf16 v[108:111], v[112:115], a[148:151], v[108:111]\n"
    "ds_read_b128 v[92:95], v137 offset:1280\n"
    "v_mfma_f32_16x16x32_bf16 v[108:111], v[112:115], a[152:155], v[108:111]\n"
    "ds_read_b128 v[96:99], v137 offset:17408\n"
    "v_mfma_f32_16x16x32_bf16 v[108:111], v[112:115], a[156:159], v[108:111]\n"
    "buffer_load_dwordx4 v17, s[20:23], s57 offen offset:1024 lds\n"
    "ds_read_b128 v[100:103], v137 offset:17664\n"
    "s_waitcnt lgkmcnt(7)\n"
    "v_mfma_f32_16x16x32_bf16 v[40:43], a[192:195], v[72:75], 0\n"
    "ds_read_b128 v[72:75], v137 offset:2048\n"
    "s_waitcnt lgkmcnt(7)\n"
    "v_mfma_f32_16x16x32_bf16 v[44:47], a[192:195], v[76:79], 0\n"
    "ds_read_b128 v[76:79], v137 offset:2304\n"
    "s_waitcnt lgkmcnt(7)\n"
    "v_mfma_f32_16x16x32_bf16 v[40:43], a[196:199], v[80:83], v[40:43]\n"
    "ds_read_b128 v[80:83], v137 offset:18432\n"
    "buffer_load_dwordx4 v24, s[20:23], s57 offen offset:2048 lds\n"
    "s_waitcnt lgkmcnt(6)\n"
    "v_mfma_f32_16x16x32_bf16 v[44:47], a[196:199], v[84:87], v[44:47]\n"
    "ds_read_b128 v[84:87], v137 offset:18688\n"
    "v_mfma_f32_16x16x32_bf16 v[40:43], a[200:203], v[88:91], v[40:43]\n"
    "s_mov_b32 s57, 0x102000\n"
    "s_add_u32 m0, s45, 0x6000\n"
    "ds_read_b128 v[88:91], v137 offset:3072\n"
    "buffer_load_dwordx4 v5, s[20:23], s57 offen lds\n"
    "s_waitcnt lgkmcnt(5)\n"
    "v_mfma_f32_16x16x32_bf16 v[44:47], a[200:203], v[92:95], v[44:47]\n"
    "ds_read_b128 v[92:95], v137 offset:3328\n"
    "v_mfma_f32_16x16x32_bf16 v[40:43], a[204:207], v[96:99], v[40:43]\n"
    "ds_read_b128 v[96:99], v137 offset:19456\n"
    "v_mfma_f32_16x16x32_bf16 v[44:47], a[204:207], v[100:103], v[44:47]\n"
    "ds_read_b128 v[100:103], v137 offset:19712\n"
    "s_waitcnt lgkmcnt(5)\n"
    "v_mfma_f32_16x16x32_bf16 v[40:43], a[208:211], v[72:75], v[40:43]\n"
    "buffer_load_dwordx4 v17, s[20:23], s57 offen offset:1024 lds\n"
    "s_waitcnt vmcnt(8)\n"
    "s_barrier\n"
    "ds_read_b128 v[72:75], v137 offset:4096\n"
    "v_mfma_f32_16x16x32_bf16 v[44:47], a[208:211], v[76:79], v[44:47]\n"
    "ds_read_b128 v[76:79], v137 offset:4352\n"
    "v_mfma_f32_16x16x32_bf16 v[40:43], a[212:215], v[80:83], v[40:43]\n"
    "ds_read_b128 v[80:83], v137 offset:20480\n"
    "s_waitcnt lgkmcnt(4)\n"
    "v_mfma_f32_16x16x32_bf16 v[44:47], a[212:215], v[84:87], v[44:47]\n"
    "ds_read_b128 v[84:87], v137 offset:20736\n"
    "v_mfma_f32_16x16x32_bf16 v[40:43], a[216:219], v[88:91], v[40:43]\n"
    "ds_read_b128 v[88:91], v137 offset:5120\n"
    "v_mfma_f32_16x16x32_bf16 v[44:47], a[216:219], v[92:95], v[44:47]\n"
    "ds_read_b128 v[92:95], v137 offset:5376\n"
    "v_mfma_f32_16x16x32_bf16 v[40:43], a[220:223], v[96:99], v[40:43]\n"
    "ds_read_b128 v[96:99], v137 offset:21504\n"
    "s_waitcnt lgkmcnt(7)\n"
    "v_mfma_f32_16x16x32_bf16 v[44:47], a[220:223], v[100:103], v[44:47]\n"
    "buffer_load_dwordx4 v24, s[20:23], s57 offen offset:2048 lds\n"
    "ds_read_b128 v[100:103], v137 offset:21760\n"
    "s_waitcnt lgkmcnt(6)\n"
    "v_mfma_f32_16x16x32_bf16 v[40:43], a[224:227], v[72:75], v[40:43]\n"
    "ds_read_b128 v[72:75], v137 offset:6144\n"
    "v_mfma_f32_16x16x32_bf16 v[44:47], a[224:227], v[76:79], v[44:47]\n"
    "ds_read_b128 v[76:79], v137 offset:6400\n"
    "s_waitcnt lgkmcnt(6)\n"
    "v_mfma_f32_16x16x32_bf16 v[40:43], a[228:231], v[80:83], v[40:43]\n"
    "buffer_atomic_or v16, v33, s[28:31], s4 offen offset:256 sc0 sc1\n"
    "ds_read_b128 v[80:83], v137 offset:22528\n"
    "s_mov_b32 s57, 0x117800\n"
    "s_add_u32 m0, s45, 0x9000\n"
    "v_mfma_f32_16x16x32_bf16 v[44:47], a[228:231], v[84:87], v[44:47]\n"
    "buffer_load_dwordx4 v5, s[20:23], s57 offen lds\n"
    "ds_read_b128 v[84:87], v137 offset:22784\n"
    "s_waitcnt lgkmcnt(5)\n"
    "v_mfma_f32_16x16x32_bf16 v[40:43], a[232:235], v[88:91], v[40:43]\n"
    "ds_read_b128 v[88:91], v137 offset:7168\n"
    "v_mfma_f32_16x16x32_bf16 v[44:47], a[232:235], v[92:95], v[44:47]\n"
    "buffer_load_dwordx4 v17, s[20:23], s57 offen offset:1024 lds\n"
    "v_mfma_f32_16x16x32_bf16 v[40:43], a[236:239], v[96:99], v[40:43]\n"
    "ds_read_b128 v[92:95], v137 offset:7424\n"
    "ds_read_b128 v[96:99], v137 offset:23552\n"
    "s_waitcnt lgkmcnt(5)\n"
    "v_mfma_f32_16x16x32_bf16 v[44:47], a[236:239], v[100:103], v[44:47]\n"
    "ds_read_b128 v[100:103], v137 offset:23808\n"
    "v_mfma_f32_16x16x32_bf16 v[40:43], a[240:243], v[72:75], v[40:43]\n"
    "v_mfma_f32_16x16x32_bf16 v[44:47], a[240:243], v[76:79], v[44:47]\n"
    "buffer_load_dwordx4 v24, s[20:23], s57 offen offset:2048 lds\n"
    "s_waitcnt lgkmcnt(4)\n"
    "v_mfma_f32_16x16x32_bf16 v[40:43], a[244:247], v[80:83], v[40:43]\n"
    "v_mfma_f32_16x16x32_bf16 v[44:47], a[244:247], v[84:87], v[44:47]\n"
    "s_waitcnt lgkmcnt(3)\n"
    "v_mfma_f32_16x16x32_bf16 v[40:43], a[248:251], v[88:91], v[40:43]\n"
    "s_waitcnt lgkmcnt(2)\n"
    "s_mov_b32 s57, 0x81000\n"
    "s_add_u32 m0, s45, 0xc000\n"
    "v_mfma_f32_16x16x32_bf16 v[44:47], a[248:251], v[92:95], v[44:47]\n"
    "buffer_load_dwordx4 v5, s[20:23], s57 offen lds\n"
    "s_waitcnt lgkmcnt(0)\n"
    "v_mfma_f32_16x16x32_bf16 v[40:43], a[252:255], v[96:99], v[40:43]\n"
    "v_mfma_f32_16x16x32_bf16 v[44:47], a[252:255], v[100:103], v[44:47]\n"
    "buffer_load_dwordx4 v17, s[20:23], s57 offen offset:1024 lds\n"
    "s_nop 5\n"
    "v_pk_add_f32 v[48:49], v[40:41], v[48:49]\n"
    "v_pk_add_f32 v[50:51], v[42:43], v[50:51]\n"
    "v_pk_mul_f32 v[60:61], v[48:49], v[48:49]\n"
    "v_pk_add_f32 v[56:57], v[48:49], v[50:51]\n"
    "v_pk_add_f32 v[52:53], v[44:45], v[52:53]\n"
    "v_pk_add_f32 v[54:55], v[46:47], v[54:55]\n"
    "v_pk_mul_f32 v[62:63], v[52:53], v[52:53]\n"
    "v_pk_add_f32 v[58:59], v[52:53], v[54:55]\n"
    "v_pk_fma_f32 v[60:61], v[50:51], v[50:51], v[60:61]\n"
    "v_pk_fma_f32 v[62:63], v[54:55], v[54:55], v[62:63]\n"
    "v_add_f32 v68, v56, v57\n"
    "v_add_f32 v69, v58, v59\n"
    "v_add_f32 v70, v60, v61\n"
    "v_add_f32 v71, v62, v63\n"
    "v_cvt_pk_bf16_f32 v64, v48, v49\n"
    "v_cvt_pk_bf16_f32 v65, v50, v51\n"
    "v_cvt_pk_bf16_f32 v66, v52, v53\n"
    "buffer_load_dwordx4 v24, s[20:23], s57 offen offset:2048 lds\n"
    "v_cvt_pk_bf16_f32 v67, v54, v55\n"
    "v_permlane32_swap_b32 v68, v70\n"
    "v_permlane32_swap_b32 v69, v71\n"
    "v_add_f32 v68, v68, v70\n"
    "v_add_f32 v69, v69, v71\n"
    "v_mov_b32 v8, v64\n"
    "v_mov_b32 v9, v65\n"
    "v_mov_b32 v10, v66\n"
    "v_mov_b32 v11, v67\n"
    "v_permlane16_swap_b32 v68, v69\n"
    "v_permlane32_swap_b32 v64, v66\n"
    "v_permlane32_swap_b32 v65, v67\n"
    "v_add_f32 v68, v68, v69\n"
    "v_lshlrev_b32 v48, 16, v12\n"
    "v_and_b32 v49, 0xffff0000, v12\n"
    "v_lshlrev_b32 v52, 16, v14\n"
    "v_and_b32 v53, 0xffff0000, v14\n"
    "v_lshlrev_b32 v50, 16, v13\n"
    "v_and_b32 v51, 0xffff0000, v13\n"
    "v_lshlrev_b32 v54, 16, v15\n"
    "v_and_b32 v55, 0xffff0000, v15\n"
    "buffer_store_dwordx4 v[64:67], v143, s[28:31], s92 offen sc1\n"
    "s_add_u32 s92, s93, 0x70000\n"
    "buffer_store_dword v68, v150, s[28:31], s89 offen sc1\n"
    "s_waitcnt vmcnt(8)\n"
    "v_cmp_eq_u32 vcc, v16, v27\n"
    "s_nop 0\n"
    "s_or_b64 vcc, vcc, s[98:99]\n"
    "s_cmp_eq_u64 vcc, -1\n"
    "s_cbranch_scc0 L_spin_ob_%=\n"
    "L_ok_ob_%=:\n"
    "s_lshl_b32 s89, s76, 8\n"
    "s_add_u32 s89, s89, s61\n"
    "s_add_u32 m0, s47, 0x20000\n"
    "s_nop 0\n"
    "buffer_load_dwordx4 v2, s[28:31], s64 offen sc1 lds\n"
    "buffer_load_dwordx4 v2, s[28:31], s64 offen offset:1024 sc1 lds\n"
    "buffer_load_dwordx4 v2, s[28:31], s64 offen offset:2048 sc1 lds\n"
    "s_waitcnt vmcnt(8)\n"
    "s_barrier\n"
    "ds_read_b64_tr_b16 a[32:33], v138\n"
    "ds_read_b64_tr_b16 a[34:35], v138 offset:3072\n"
    "ds_read_b64_tr_b16 a[160:161], v140\n"
    "ds_read_b64_tr_b16 a[162:163], v140 offset:3072\n"
    "ds_read_b64_tr_b16 a[96:97], v139\n"
    "ds_read_b64_tr_b16 a[98:99], v139 offset:3072\n"
    "buffer_load_dwordx4 v2, s[28:31], s64 offen offset:3072 sc1 lds\n"
    "ds_read_b64_tr_b16 a[36:37], v138 offset:6144\n"
    "ds_read_b64_tr_b16 a[38:39], v138 offset:9216\n"
    "ds_read_b64_tr_b16 a[164:165], v140 offset:6144\n"
    "ds_read_b64_tr_b16 a[166:167], v140 offset:9216\n"
    "ds_read_b64_tr_b16 a[100:101], v139 offset:6144\n"
    "ds_read_b64_tr_b16 a[102:103], v139 offset:9216\n"
    "ds_read_b64_tr_b16 a[40:41], v138 offset:12288\n"
    "s_add_u32 m0, s47, 0x21000\n"
    "ds_read_b64_tr_b16 a[42:43], v138 offset:15360\n"
    "buffer_load_dwordx4 v2, s[28:31], s65 offen sc1 lds\n"
    "ds_read_b64_tr_b16 a[168:169], v140 offset:12288\n"
    "ds_read_b64_tr_b16 a[170:171], v140 offset:15360\n"
    "ds_read_b64_tr_b16 a[104:105], v139 offset:12288\n"
    "ds_read_b64_tr_b16 a[106:107], v139 offset:15360\n"
    "s_waitcnt lgkmcnt(14)\n"
    "v_mfma_f32_16x16x32_bf16 v[104:107], v[112:115], a[32:35], v[104:107]\n"
    "ds_read_b64_tr_b16 a[44:45], v138 offset:18432\n"
    "ds_read_b64_tr_b16 a[46:47], v138 offset:21504\n"
    "ds_read_b64_tr_b16 a[172:173], v140 offset:18432\n"
    "s_waitcnt lgkmcnt(14)\n"
    "v_mfma_f32_16x16x32_bf16 v[104:107], v[112:115], a[96:99], v[104:107]\n"
    "buffer_load_dwordx4 v2, s[28:31], s65 offen offset:1024 sc1 lds\n"
    "ds_read_b64_tr_b16 a[174:175], v140 offset:21504\n"
    "ds_read_b64_tr_b16 a[108:109], v139 offset:18432\n"
    "ds_read_b64_tr_b16 a[110:111], v139 offset:21504\n"
    "s_waitcnt lgkmcnt(13)\n"
    "v_mfma_f32_16x16x32_bf16 v[104:107], v[112:115], a[36:39], v[104:107]\n"
    "s_waitcnt lgkmcnt(11)\n"
    "v_mfma_f32_16x16x32_bf16 v[104:107], v[112:115], a[100:103], v[104:107]\n"
    "s_waitcnt lgkmcnt(8)\n"
    "v_mfma_f32_16x16x32_bf16 v[104:107], v[112:115], a[40:43], v[104:107]\n"
    "s_waitcnt lgkmcnt(6)\n"
    "v_mfma_f32_16x16x32_bf16 v[104:107], v[112:115], a[104:107], v[104:107]\n"
    "s_waitcnt lgkmcnt(3)\n"
    "v_mfma_f32_16x16x32_bf16 v[104:107], v[112:115], a[44:47], v[104:107]\n"
    "s_waitcnt lgkmcnt(0)\n"
    "v_mfma_f32_16x16x32_bf16 v[104:107], v[112:115], a[108:111], v[104:107]\n"
    "v_mfma_f32_16x16x32_bf16 v[108:111], v[112:115], a[160:163], v[108:111]\n"
    "buffer_load_dwordx4 v2, s[28:31], s65 offen offset:2048 sc1 lds\n"
    "v_mfma_f32_16x16x32_bf16 v[108:111], v[112:115], a[164:167], v[108:111]\n"
    "s_waitcnt vmcnt(7)\n"
    "buffer_store_dword v27, v31, s[28:31], s3 offen offset:384 sc1\n"
    "v_mov_b32 v28, 0\n"
    "v_mfma_f32_16x16x32_bf16 v[108:111], v[112:115], a[168:171], v[108:111]\n"
    "v_mfma_f32_16x16x32_bf16 v[108:111], v[112:115], a[172:175], v[108:111]\n"
    "buffer_load_dwordx4 v2, s[28:31], s65 offen offset:3072 sc1 lds\n"
    "s_mov_b32 s57, 0x96800\n"
    "s_add_u32 m0, s45, 0xf000\n"
    "s_nop 0\n"
    "buffer_load_dwordx4 v5, s[20:23], s57 offen lds\n"
    "buffer_load_dwordx4 v17, s[20:23], s57 offen offset:1024 lds\n"
    "buffer_load_dwordx4 v24, s[20:23], s57 offen offset:2048 lds\n"
    "s_waitcnt vmcnt(8)\n"
    "s_barrier\n"
    "ds_read_b128 v[72:75], v137 offset:32768\n"
    "ds_read_b128 v[76:79], v137 offset:33024\n"
    "s_mov_b32 s57, 0x12d000\n"
    "s_add_u32 m0, s45, 0x12000\n"
    "ds_read_b128 v[80:83], v137 offset:49152\n"
    "buffer_load_dwordx4 v5, s[20:23], s57 offen lds\n"
    "ds_read_b128 v[84:87], v137 offset:49408\n"
    "ds_read_b128 v[88:91], v137 offset:33792\n"
    "ds_read_b128 v[92:95], v137 offset:34048\n"
    "ds_read_b128 v[96:99], v137 offset:50176\n"
    "ds_read_b128 v[100:103], v137 offset:50432\n"
    "buffer_load_dwordx4 v17, s[20:23], s57 offen offset:1024 lds\n"
    "s_waitcnt lgkmcnt(7)\n"
    "v_mfma_f32_16x16x32_bf16 v[40:43], a[192:195], v[72:75], 0\n"
    "ds_read_b128 v[72:75], v137 offset:34816\n"
    "s_waitcnt lgkmcnt(7)\n"
    "v_mfma_f32_16x16x32_bf16 v[44:47], a[192:195], v[76:79], 0\n"
    "ds_read_b128 v[76:79], v137 offset:35072\n"
    "s_waitcnt lgkmcnt(7)\n"
    "v_mfma_f32_16x16x32_bf16 v[40:43], a[196:199], v[80:83], v[40:43]\n"
    "ds_read_b128 v[80:83], v137 offset:51200\n"
    "s_waitcnt lgkmcnt(7)\n"
    "v_mfma_f32_16x16x32_bf16 v[44:47], a[196:199], v[84:87], v[44:47]\n"
    "buffer_load_dwordx4 v24, s[20:23], s57 offen offset:2048 lds\n"
    "ds_read_b128 v[84:87], v137 offset:51456\n"
    "s_waitcnt lgkmcnt(6)\n"
    "v_mfma_f32_16x16x32_bf16 v[40:43], a[200:203], v[88:91], v[40:43]\n"
    "ds_read_b128 v[88:91], v137 offset:35840\n"
    "v_mfma_f32_16x16x32_bf16 v[44:47], a[200:203], v[92:95], v[44:47]\n"
    "s_mov_b32 s57, 0x142800\n"
    "s_add_u32 m0, s45, 0x15000\n"
    "ds_read_b128 v[92:95], v137 offset:36096\n"
    "buffer_load_dwordx4 v5, s[20:23], s57 offen lds\n"
    "s_waitcnt lgkmcnt(5)\n"
    "v_mfma_f32_16x16x32_bf16 v[40:43], a[204:207], v[96:99], v[40:43]\n"
    "ds_read_b128 v[96:99], v137 offset:52224\n"
    "v_mfma_f32_16x16x32_bf16 v[44:47], a[204:207], v[100:103], v[44:47]\n"
    "ds_read_b128 v[100:103], v137 offset:52480\n"
    "v_mfma_f32_16x16x32_bf16 v[40:43], a[208:211], v[72:75], v[40:43]\n"
    "s_waitcnt lgkmcnt(5)\n"
    "v_mfma_f32_16x16x32_bf16 v[44:47], a[208:211], v[76:79], v[44:47]\n"
    "buffer_load_dwordx4 v17, s[20:23], s57 offen offset:1024 lds\n"
    "s_waitcnt vmcnt(8)\n"
    "s_barrier\n"
    "ds_read_b128 v[72:75], v137 offset:36864\n"
    "ds_read_b128 v[76:79], v137 offset:37120\n"
    "v_mfma_f32_16x16x32_bf16 v[40:43], a[212:215], v[80:83], v[40:43]\n"
    "buffer_load_dwordx4 v24, s[20:23], s57 offen offset:2048 lds\n"
    "ds_read_b128 v[80:83], v137 offset:53248\n"
    "s_waitcnt lgkmcnt(3)\n"
    "v_mfma_f32_16x16x32_bf16 v[44:47], a[212:215], v[84:87], v[44:47]\n"
    "ds_read_b128 v[84:87], v137 offset:53504\n"
    "v_lshlrev_b32 v24, 4, v38\n"
    "v_mfma_f32_16x16x32_bf16 v[40:43], a[216:219], v[88:91], v[40:43]\n"
    "ds_read_b128 v[88:91], v137 offset:37888\n"
    "v_mfma_f32_16x16x32_bf16 v[44:47], a[216:219], v[92:95], v[44:47]\n"
    "ds_read_b128 v[92:95], v137 offset:38144\n"
    "buffer_atomic_or v28, v33, s[28:31], s4 offen offset:384 sc0 sc1\n"
    "v_mfma_f32_16x16x32_bf16 v[40:43], a[220:223], v[96:99], v[40:43]\n"
    "ds_read_b128 v[96:99], v137 offset:54272\n"
    "v_mfma_f32_16x16x32_bf16 v[44:47], a[220:223], v[100:103], v[44:47]\n"
    "ds_read_b128 v[100:103], v137 offset:54528\n"
    "s_waitcnt lgkmcnt(7)\n"
    "v_mfma_f32_16x16x32_bf16 v[40:43], a[224:227], v[72:75], v[40:43]\n"
    "ds_read_b128 v[72:75], v137 offset:38912\n"
    "s_waitcnt lgkmcnt(6)\n"
    "v_mfma_f32_16x16x32_bf16 v[44:47], a[224:227], v[76:79], v[44:47]\n"
    "buffer_load_ushort v22, v154, s[36:39], s53 offen\n"
    "ds_read_b128 v[76:79], v137 offset:39168\n"
    "v_mfma_f32_16x16x32_bf16 v[40:43], a[228:231], v[80:83], v[40:43]\n"
    "ds_read_b128 v[80:83], v137 offset:55296\n"
    "v_lshrrev_b32 v154, 4, v34\n"
    "s_waitcnt lgkmcnt(6)\n"
    "v_mfma_f32_16x16x32_bf16 v[44:47], a[228:231], v[84:87], v[44:47]\n"
    "ds_read_b128 v[84:87], v137 offset:55552\n"
    "v_lshl_add_u32 v154, v154, 24, v155\n"
    "v_mfma_f32_16x16x32_bf16 v[40:43], a[232:235], v[88:91], v[40:43]\n"
    "ds_read_b128 v[88:91], v137 offset:39936\n"
    "v_lshrrev_b32 v155, 4, v34\n"
    "s_waitcnt lgkmcnt(6)\n"
    "v_mfma_f32_16x16x32_bf16 v[44:47], a[232:235], v[92:95], v[44:47]\n"
    "ds_read_b128 v[92:95], v137 offset:40192\n"
    "buffer_load_ushort v23, v154, s[36:39], s52 offen offset:256\n"
    "v_mfma_f32_16x16x32_bf16 v[40:43], a[236:239], v[96:99], v[40:43]\n"
    "ds_read_b128 v[96:99], v137 offset:56320\n"
    "v_and_b32 v154, 15, v34\n"
    "s_waitcnt lgkmcnt(6)\n"
    "v_mfma_f32_16x16x32_bf16 v[44:47], a[236:239], v[100:103], v[44:47]\n"
    "ds_read_b128 v[100:103], v137 offset:56576\n"
    "v_lshl_add_u32 v154, v154, 2, v155\n"
    "v_mfma_f32_16x16x32_bf16 v[40:43], a[240:243], v[72:75], v[40:43]\n"
    "v_lshl_add_u32 v148, v154, 2, s54\n"
    "v_and_b32 v154, 0x7c, v150\n"
    "s_waitcnt lgkmcnt(5)\n"
    "s_mov_b32 s57, 0xb8000\n"
    "s_add_u32 m0, s46, 0x0\n"
    "v_mfma_f32_16x16x32_bf16 v[44:47], a[240:243], v[76:79], v[44:47]\n"
    "buffer_load_dwordx4 v6, s[24:27], s57 offen lds\n"
    "v_mfma_f32_16x16x32_bf16 v[40:43], a[244:247], v[80:83], v[40:43]\n"
    "v_add_u32 v149, 0x14000, v154\n"
    "v_cmp_gt_u32 s[52:53], 32, v34\n"
    "s_waitcnt lgkmcnt(2)\n"
    "v_mfma_f32_16x16x32_bf16 v[44:47], a[244:247], v[84:87], v[44:47]\n"
    "s_add_u32 s54, s78, s79\n"
    "s_add_u32 s40, s54, 0x80000\n"
    "v_mfma_f32_16x16x32_bf16 v[40:43], a[248:251], v[88:91], v[40:43]\n"
    "s_add_u32 s41, s54, 0x81000\n"
    "s_add_u32 s58, s54, 0xb0000\n"
    "v_mfma_f32_16x16x32_bf16 v[44:47], a[248:251], v[92:95], v[44:47]\n"
    "buffer_load_dwordx4 v3, s[24:27], s57 offen offset:1024 lds\n"
    "s_waitcnt lgkmcnt(0)\n"
    "v_mfma_f32_16x16x32_bf16 v[40:43], a[252:255], v[96:99], v[40:43]\n"
    "s_add_u32 s43, s54, 0x85000\n"
    "s_add_u32 s56, s54, 0x89000\n"
    "v_mfma_f32_16x16x32_bf16 v[44:47], a[252:255], v[100:103], v[44:47]\n"
    "s_add_u32 s42, s54, 0x84000\n"
    "s_add_u32 s44, s54, 0x88000\n"
    "s_add_u32 s59, s54, 0xb1000\n"
    "s_add_u32 s71, s54, 0xb5000\n"
    "s_nop 0\n"
    "v_pk_add_f32 v[48:49], v[40:41], v[48:49]\n"
    "buffer_load_dwordx4 v4, s[24:27], s57 offen offset:2048 lds\n"
    "s_nop 0\n"
    "v_pk_add_f32 v[52:53], v[44:45], v[52:53]\n"
    "v_pk_add_f32 v[50:51], v[42:43], v[50:51]\n"
    "v_pk_add_f32 v[54:55], v[46:47], v[54:55]\n"
    "v_pk_mul_f32 v[60:61], v[48:49], v[48:49]\n"
    "v_pk_mul_f32 v[62:63], v[52:53], v[52:53]\n"
    "v_pk_add_f32 v[56:57], v[48:49], v[50:51]\n"
    "v_pk_add_f32 v[58:59], v[52:53], v[54:55]\n"
    "v_pk_fma_f32 v[60:61], v[50:51], v[50:51], v[60:61]\n"
    "v_pk_fma_f32 v[62:63], v[54:55], v[54:55], v[62:63]\n"
    "v_add_f32 v68, v56, v57\n"
    "v_add_f32 v69, v58, v59\n"
    "v_add_f32 v70, v60, v61\n"
    "v_add_f32 v71, v62, v63\n"
    "v_cvt_pk_bf16_f32 v64, v48, v49\n"
    "v_cvt_pk_bf16_f32 v65, v50, v51\n"
    "buffer_load_dwordx4 v30, s[24:27], s57 offen offset:3072 lds\n"
    "v_permlane32_swap_b32 v68, v70\n"
    "v_permlane32_swap_b32 v69, v71\n"
    "v_add_f32 v68, v68, v70\n"
    "v_add_f32 v69, v69, v71\n"
    "v_cvt_pk_bf16_f32 v66, v52, v53\n"
    "v_cvt_pk_bf16_f32 v67, v54, v55\n"
    "v_mov_b32 v12, v64\n"
    "v_mov_b32 v13, v65\n"
    "v_mov_b32 v14, v66\n"
    "v_mov_b32 v15, v67\n"
    "v_permlane16_swap_b32 v68, v69\n"
    "s_add_u32 m0, s46, 0x1000\n"
    "v_permlane32_swap_b32 v64, v66\n"
    "buffer_load_dwordx4 v7, s[24:27], s57 offen lds\n"
    "v_add_f32 v68, v68, v69\n"
    "s_waitcnt vmcnt(7)\n"
    "v_cmp_eq_u32 vcc, v28, v27\n"
    "s_nop 0\n"
    "s_or_b64 vcc, vcc, s[98:99]\n"
    "s_cmp_eq_u64 vcc, -1\n"
    "s_cbranch_scc0 L_spin_ha_%=\n"
    "L_ok_ha_%=:\n"
    "s_add_u32 m0, s47, 0x18000\n"
    "s_nop 0\n"
    "buffer_load_dwordx4 v2, s[28:31], s66 offen sc1 lds\n"
    "v_permlane32_swap_b32 v65, v67\n"
    "s_barrier\n"
    "ds_read_b64_tr_b16 a[48:49], v151\n"
    "buffer_store_dwordx4 v[64:67], v143, s[28:31], s92 offen sc1\n"
    "ds_read_b64_tr_b16 a[50:51], v151 offset:3072\n"
    "ds_read_b64_tr_b16 a[176:177], v153\n"
    "ds_read_b64_tr_b16 a[178:179], v153 offset:3072\n"
    "ds_read_b64_tr_b16 a[112:113], v152\n"
    "ds_read_b64_tr_b16 a[114:115], v152 offset:3072\n"
    "s_waitcnt vmcnt(8)\n"
    "v_lshlrev_b32 v22, 16, v22\n"
    "ds_read_b64_tr_b16 a[52:53], v151 offset:6144\n"
    "ds_read_b64_tr_b16 a[54:55], v151 offset:9216\n"
    "ds_read_b64_tr_b16 a[180:181], v153 offset:6144\n"
    "v_cvt_pk_bf16_f32 v118, v22, 0\n"
    "ds_read_b64_tr_b16 a[182:183], v153 offset:9216\n"
    "ds_read_b64_tr_b16 a[116:117], v152 offset:6144\n"
    "s_add_u32 s73, s54, 0xb9000\n"
    "buffer_store_dword v68, v150, s[28:31], s89 offen sc1\n"
    "ds_read_b64_tr_b16 a[118:119], v152 offset:9216\n"
    "ds_read_b64_tr_b16 a[56:57], v151 offset:12288\n"
    "ds_read_b64_tr_b16 a[58:59], v151 offset:15360\n"
    "ds_read_b64_tr_b16 a[184:185], v153 offset:12288\n"
    "s_waitcnt lgkmcnt(13)\n"
    "v_mfma_f32_16x16x32_bf16 v[104:107], v[112:115], a[48:51], v[104:107]\n"
    "ds_read_b64_tr_b16 a[186:187], v153 offset:15360\n"
    "ds_read_b64_tr_b16 a[120:121], v152 offset:12288\n"
    "ds_read_b64_tr_b16 a[122:123], v152 offset:15360\n"
    "s_waitcnt lgkmcnt(14)\n"
    "v_mfma_f32_16x16x32_bf16 v[108:111], v[112:115], a[176:179], v[108:111]\n"
    "ds_read_b64_tr_b16 a[60:61], v151 offset:18432\n"
    "ds_read_b64_tr_b16 a[62:63], v151 offset:21504\n"
    "ds_read_b64_tr_b16 a[188:189], v153 offset:18432\n"
    "s_waitcnt lgkmcnt(14)\n"
    "v_mfma_f32_16x16x32_bf16 v[104:107], v[112:115], a[112:115], v[104:107]\n"
    "ds_read_b64_tr_b16 a[190:191], v153 offset:21504\n"
    "ds_read_b64_tr_b16 a[124:125], v152 offset:18432\n"
    "ds_read_b64_tr_b16 a[126:127], v152 offset:21504\n"
    "s_waitcnt lgkmcnt(14)\n"
    "v_mfma_f32_16x16x32_bf16 v[104:107], v[112:115], a[52:55], v[104:107]\n"
    "s_waitcnt vmcnt(8)\n"
    "v_lshlrev_b32 v23, 16, v23\n"
    "buffer_load_dwordx4 v2, s[28:31], s66 offen offset:1024 sc1 lds\n"
    "s_waitcnt lgkmcnt(11)\n"
    "v_mfma_f32_16x16x32_bf16 v[104:107], v[112:115], a[116:119], v[104:107]\n"
    "v_cvt_pk_bf16_f32 v122, v23, 0\n"
    "s_lshl_b32 s89, s76, 10\n"
    "s_waitcnt lgkmcnt(8)\n"
    "v_mfma_f32_16x16x32_bf16 v[104:107], v[112:115], a[56:59], v[104:107]\n"
    "s_add_u32 s93, s89, s40\n"
    "s_cmp_ge_u32 s76, 3\n"
    "s_cselect_b32 s89, s91, 0\n"
    "v_mfma_f32_16x16x32_bf16 v[108:111], v[112:115], a[180:183], v[108:111]\n"
    "s_add_u32 s92, s93, 0x8000\n"
    "s_add_u32 s90, s93, 0x4000\n"
    "s_add_u32 m0, s47, 0x19000\n"
    "s_nop 0\n"
    "buffer_load_dwordx4 v2, s[28:31], s67 offen sc1 lds\n"
    "s_waitcnt lgkmcnt(0)\n"
    "s_barrier\n"
    "v_mfma_f32_16x16x32_bf16 v[104:107], v[112:115], a[120:123], v[104:107]\n"
    "buffer_load_dwordx4 v2, s[28:31], s67 offen offset:1024 sc1 lds\n"
    "v_mfma_f32_16x16x32_bf16 v[108:111], v[112:115], a[184:187], v[108:111]\n"
    "s_add_u32 s92, s92, s89\n"
    "s_lshl_b32 s89, s76, 10\n"
    "v_mfma_f32_16x16x32_bf16 v[104:107], v[112:115], a[60:63], v[104:107]\n"
    "s_add_u32 s70, s54, 0xb4000\n"
    "s_add_u32 s72, s54, 0xb8000\n"
    "v_mfma_f32_16x16x32_bf16 v[108:111], v[112:115], a[188:191], v[108:111]\n"
    "s_add_u32 m0, s47, 0x18000\n"
    "v_mfma_f32_16x16x32_bf16 v[104:107], v[112:115], a[124:127], v[104:107]\n"
    "buffer_load_dwordx4 v2, s[28:31], s66 offen offset:2048 sc1 lds\n"
    "s_nop 4\n"
    "v_cndmask_b32 v108, 0, v108, s[52:53]\n"
    "v_cvt_pk_bf16_f32 v121, v108, v23\n"
    "v_lshlrev_b32 v109, 16, v121\n"
    "v_sub_f32 v109, v108, v109\n"
    "v_cndmask_b32 v104, 0, v104, s[52:53]\n"
    "v_cvt_pk_bf16_f32 v120, v108, v109\n"
    "v_cvt_pk_bf16_f32 v117, v104, v22\n"
    "s_lshl_b32 s52, s76, 7\n"
    "v_lshlrev_b32 v105, 16, v117\n"
    "s_add_u32 s53, s47, 0x18000\n"
    "v_sub_f32 v105, v104, v105\n"
    "v_add_u32 v136, s53, v136\n"
    "v_cvt_pk_bf16_f32 v116, v104, v105\n"
    "s_lshl_b32 s53, s74, 1\n"
    "v_xor_b32 v137, 32, v136\n"
    "buffer_load_dwordx4 v2, s[28:31], s66 offen offset:3072 sc1 lds\n"
    "v_add_u32 v143, s53, v37\n"
    "v_xor_b32 v138, 64, v136\n"
    "v_xor_b32 v143, v143, v38\n"
    "v_xor_b32 v139, 96, v136\n"
    "v_lshlrev_b32 v143, 4, v143\n"
    "v_lshlrev_b32 v37, 1, v38\n"
    "s_add_u32 s53, s85, 0x18000\n"
    "s_add_u32 m0, s47, 0x19000\n"
    "s_nop 0\n"
    "buffer_load_dwordx4 v2, s[28:31], s67 offen offset:2048 sc1 lds\n"
    "buffer_load_dwordx4 v2, s[28:31], s67 offen offset:3072 sc1 lds\n"
    "s_waitcnt vmcnt(7)\n"
    "buffer_store_dword v27, v31, s[28:31], s3 offen offset:512 sc1\n"
    "v_mov_b32 v16, 0\n"
    "buffer_load_dwordx4 v[128:131], v1, s[28:31], s60 offen sc1\n"
    "buffer_load_dwordx4 v[132:135], v1, s[28:31], s60 offen offset:1024 sc1\n"
    "s_waitcnt vmcnt(7)\n"
    "s_barrier\n"
    "s_add_u32 m0, s46, 0x1000\n"
    "ds_read_b128 v[88:91], v144\n"
    "buffer_load_dwordx4 v32, s[24:27], s57 offen offset:1024 lds\n"
    "ds_read_b128 v[92:95], v144 offset:8192\n"
    "s_mov_b32 s57, 0xe0000\n"
    "s_add_u32 m0, s46, 0x6000\n"
    "ds_read_b128 v[96:99], v144 offset:16384\n"
    "buffer_load_dwordx4 v6, s[24:27], s57 offen lds\n"
    "ds_read_b128 v[100:103], v144 offset:24576\n"
    "ds_read_b128 v[72:75], v145\n"
    "ds_read_b128 v[76:79], v145 offset:8192\n"
    "ds_read_b128 v[80:83], v145 offset:16384\n"
    "s_waitcnt lgkmcnt(6)\n"
    "v_mfma_f32_32x32x16_bf16 v[56:71], a[0:3], v[88:91], 0\n"
    "ds_read_b128 v[84:87], v145 offset:24576\n"
    "buffer_load_dwordx4 v3, s[24:27], s57 offen offset:1024 lds\n"
    "v_mfma_f32_32x32x16_bf16 v[40:55], a[128:131], v[88:91], 0\n"
    "ds_read_b128 v[88:91], v144 offset:1024\n"
    "s_waitcnt lgkmcnt(6)\n"
    "v_mfma_f32_32x32x16_bf16 v[56:71], a[4:7], v[92:95], v[56:71]\n"
    "v_mfma_f32_32x32x16_bf16 v[40:55], a[132:135], v[92:95], v[40:55]\n"
    "buffer_load_dwordx4 v4, s[24:27], s57 offen offset:2048 lds\n"
    "ds_read_b128 v[92:95], v144 offset:9216\n"
    "v_mfma_f32_32x32x16_bf16 v[56:71], a[8:11], v[96:99], v[56:71]\n"
    "v_mfma_f32_32x32x16_bf16 v[40:55], a[136:139], v[96:99], v[40:55]\n"
    "buffer_load_dwordx4 v30, s[24:27], s57 offen offset:3072 lds\n"
    "ds_read_b128 v[96:99], v144 offset:17408\n"
    "s_waitcnt lgkmcnt(2)\n"
    "v_mfma_f32_32x32x16_bf16 v[56:71], a[12:15], v[100:103], v[56:71]\n"
    "s_waitcnt vmcnt(8)\n"
    "s_barrier\n"
    "s_add_u32 m0, s46, 0x7000\n"
    "s_nop 0\n"
    "buffer_load_dwordx4 v7, s[24:27], s57 offen lds\n"
    "v_mfma_f32_32x32x16_bf16 v[40:55], a[140:143], v[100:103], v[40:55]\n"
    "ds_read_b128 v[100:103], v144 offset:25600\n"
    "v_mfma_f32_32x32x16_bf16 v[56:71], a[16:19], v[88:91], v[56:71]\n"
    "buffer_load_dwordx4 v32, s[24:27], s57 offen offset:1024 lds\n"
    "v_mfma_f32_32x32x16_bf16 v[40:55], a[144:147], v[88:91], v[40:55]\n"
    "ds_read_b128 v[88:91], v144 offset:2048\n"
    "s_waitcnt lgkmcnt(2)\n"
    "s_mov_b32 s57, 0x108000\n"
    "s_add_u32 m0, s46, 0xc000\n"
    "v_mfma_f32_32x32x16_bf16 v[56:71], a[20:23], v[92:95], v[56:71]\n"
    "buffer_load_dwordx4 v6, s[24:27], s57 offen lds\n"
    "v_mfma_f32_32x32x16_bf16 v[40:55], a[148:151], v[92:95], v[40:55]\n"
    "ds_read_b128 v[92:95], v144 offset:10240\n"
    "s_waitcnt vmcnt(8)\n"
    "v_add_f32 v128, v128, v132\n"
    "v_add_f32 v129, v129, v133\n"
    "v_add_f32 v130, v130, v134\n"
    "v_add_f32 v131, v131, v135\n"
    "v_mfma_f32_32x32x16_bf16 v[56:71], a[24:27], v[96:99], v[56:71]\n"
    "v_permlane32_swap_b32 v128, v130\n"
    "v_permlane32_swap_b32 v129, v131\n"
    "v_add_f32 v128, v128, v130\n"
    "v_add_f32 v129, v129, v131\n"
    "v_mfma_f32_32x32x16_bf16 v[40:55], a[152:155], v[96:99], v[40:55]\n"
    "ds_read_b128 v[96:99], v144 offset:18432\n"
    "v_permlane16_swap_b32 v128, v129\n"
    "v_add_f32 v128, v128, v129\n"
    "ds_write_b32 v148, v128\n"
    "buffer_atomic_or v16, v33, s[28:31], s4 offen offset:512 sc0 sc1\n"
    "s_waitcnt lgkmcnt(3)\n"
    "v_mfma_f32_32x32x16_bf16 v[56:71], a[28:31], v[100:103], v[56:71]\n"
    "s_waitcnt vmcnt(8)\n"
    "ds_read_b64_tr_b16 v[160:161], v141\n"
    "ds_read_b64_tr_b16 v[162:163], v141 offset:1024\n"
    "ds_read_b64_tr_b16 v[164:165], v142\n"
    "ds_read_b64_tr_b16 v[166:167], v142 offset:1024\n"
    "ds_read_b64_tr_b16 v[168:169], v141 offset:2048\n"
    "v_mfma_f32_32x32x16_bf16 v[40:55], a[156:159], v[100:103], v[40:55]\n"
    "buffer_load_dwordx4 v3, s[24:27], s57 offen offset:1024 lds\n"
    "ds_read_b128 v[100:103], v144 offset:26624\n"
    "ds_read_b64_tr_b16 v[170:171], v141 offset:3072\n"
    "ds_read_b64_tr_b16 v[172:173], v142 offset:2048\n"
    "v_mfma_f32_32x32x16_bf16 v[56:71], a[32:35], v[88:91], v[56:71]\n"
    "ds_read_b64_tr_b16 v[174:175], v142 offset:3072\n"
    "ds_read_b64_tr_b16 v[176:177], v141 offset:4096\n"
    "ds_read_b64_tr_b16 v[178:179], v141 offset:5120\n"
    "ds_read_b64_tr_b16 v[180:181], v142 offset:4096\n"
    "v_mfma_f32_32x32x16_bf16 v[40:55], a[160:163], v[88:91], v[40:55]\n"
    "buffer_load_dwordx4 v4, s[24:27], s57 offen offset:2048 lds\n"
    "ds_read_b128 v[88:91], v144 offset:3072\n"
    "ds_read_b64_tr_b16 v[182:183], v142 offset:5120\n"
    "s_waitcnt lgkmcnt(12)\n"
    "v_mfma_f32_32x32x16_bf16 v[56:71], a[36:39], v[92:95], v[56:71]\n"
    "v_mfma_f32_32x32x16_bf16 v[40:55], a[164:167], v[92:95], v[40:55]\n"
    "buffer_load_dwordx4 v30, s[24:27], s57 offen offset:3072 lds\n"
    "ds_read_b128 v[92:95], v144 offset:11264\n"
    "v_mfma_f32_32x32x16_bf16 v[56:71], a[40:43], v[96:99], v[56:71]\n"
    "s_waitcnt lgkmcnt(1)\n"
    "s_add_u32 m0, s46, 0xd000\n"
    "v_mfma_f32_32x32x16_bf16 v[40:55], a[168:171], v[96:99], v[40:55]\n"
    "buffer_load_dwordx4 v7, s[24:27], s57 offen lds\n"
    "ds_read_b128 v[96:99], v144 offset:19456\n"
    "v_mfma_f32_32x32x16_bf16 v[56:71], a[44:47], v[100:103], v[56:71]\n"
    "v_mfma_f32_32x32x16_bf16 v[40:55], a[172:175], v[100:103], v[40:55]\n"
    "ds_read_b128 v[100:103], v144 offset:27648\n"
    "buffer_load_dwordx4 v32, s[24:27], s57 offen offset:1024 lds\n"
    "v_mfma_f32_32x32x16_bf16 v[56:71], a[48:51], v[88:91], v[56:71]\n"
    "v_mfma_f32_32x32x16_bf16 v[40:55], a[176:179], v[88:91], v[40:55]\n"
    "s_mov_b32 s57, 0x130000\n"
    "s_add_u32 m0, s46, 0x0\n"
    "ds_read_b128 v[88:91], v145 offset:1024\n"
    "buffer_load_dwordx4 v6, s[24:27], s57 offen lds\n"
    "s_waitcnt vmcnt(8)\n"
    "ds_read_b64_tr_b16 v[184:185], v141 offset:24576\n"
    "ds_read_b64_tr_b16 v[186:187], v141 offset:25600\n"
    "s_waitcnt lgkmcnt(4)\n"
    "v_mfma_f32_32x32x16_bf16 v[56:71], a[52:55], v[92:95], v[56:71]\n"
    "ds_read_b64_tr_b16 v[188:189], v142 offset:24576\n"
    "ds_read_b64_tr_b16 v[190:191], v142 offset:25600\n"
    "ds_read_b64_tr_b16 v[192:193], v141 offset:26624\n"
    "ds_read_b64_tr_b16 v[194:195], v141 offset:27648\n"
    "ds_read_b64_tr_b16 v[196:197], v142 offset:26624\n"
    "ds_read_b64_tr_b16 v[198:199], v142 offset:27648\n"
    "v_mfma_f32_32x32x16_bf16 v[40:55], a[180:183], v[92:95], v[40:55]\n"
    "ds_read_b128 v[92:95], v145 offset:9216\n"
    "buffer_load_dwordx4 v3, s[24:27], s57 offen offset:1024 lds\n"
    "ds_read_b64_tr_b16 v[200:201], v141 offset:28672\n"
    "ds_read_b64_tr_b16 v[202:203], v141 offset:29696\n"
    "v_mfma_f32_32x32x16_bf16 v[56:71], a[56:59], v[96:99], v[56:71]\n"
    "buffer_load_dwordx4 v4, s[24:27], s57 offen offset:2048 lds\n"
    "ds_read_b64_tr_b16 v[204:205], v142 offset:28672\n"
    "ds_read_b64_tr_b16 v[206:207], v142 offset:29696\n"
    "v_mfma_f32_32x32x16_bf16 v[40:55], a[184:187], v[96:99], v[40:55]\n"
    "ds_read_b128 v[96:99], v145 offset:17408\n"
    "s_waitcnt vmcnt(8)\n"
    "v_cmp_eq_u32 vcc, v16, v27\n"
    "s_nop 0\n"
    "s_or_b64 vcc, vcc, s[98:99]\n"
    "s_cmp_eq_u64 vcc, -1\n"
    "s_cbranch_scc0 L_spin_hb_%=\n"
    "L_ok_hb_%=:\n"
    "s_waitcnt lgkmcnt(9)\n"
    "v_mfma_f32_32x32x16_bf16 v[56:71], a[60:63], v[100:103], v[56:71]\n"
    "s_barrier\n"
    "ds_read2_b32 v[128:129], v149 offset0:0 offset1:32\n"
    "ds_read2_b32 v[130:131], v149 offset0:64 offset1:96\n"
    "v_mfma_f32_32x32x16_bf16 v[40:55], a[188:191], v[100:103], v[40:55]\n"
    "ds_read2_b32 v[132:133], v149 offset0:128 offset1:160\n"
    "s_waitcnt lgkmcnt(4)\n"
    "ds_read2_b32 v[134:135], v149 offset0:192 offset1:224\n"
    "s_add_u32 m0, s47, 0x20000\n"
    "v_mfma_f32_32x32x16_bf16 v[56:71], a[64:67], v[72:75], v[56:71]\n"
    "buffer_load_dwordx4 v2, s[28:31], s68 offen sc1 lds\n"
    "ds_read_b128 v[100:103], v145 offset:25600\n"
    "ds_read_b128 v[72:75], v145 offset:2048\n"
    "v_mfma_f32_32x32x16_bf16 v[56:71], a[68:71], v[76:79], v[56:71]\n"
    "ds_read_b128 v[76:79], v145 offset:10240\n"
    "v_mfma_f32_32x32x16_bf16 v[56:71], a[72:75], v[80:83], v[56:71]\n"
    "buffer_load_dwordx4 v2, s[28:31], s68 offen offset:1024 sc1 lds\n"
    "ds_read_b128 v[80:83], v145 offset:18432\n"
    "s_waitcnt lgkmcnt(6)\n"
    "v_add_f32 v128, v128, v130\n"
    "v_mfma_f32_32x32x16_bf16 v[56:71], a[76:79], v[84:87], v[56:71]\n"
    "ds_read_b128 v[84:87], v145 offset:26624\n"
    "v_add_f32 v129, v129, v131\n"
    "s_waitcnt lgkmcnt(5)\n"
    "v_add_f32 v132, v132, v134\n"
    "s_add_u32 m0, s47, 0x21000\n"
    "v_add_f32 v133, v133, v135\n"
    "buffer_load_dwordx4 v2, s[28:31], s69 offen sc1 lds\n"
    "v_add_f32 v128, v128, v132\n"
    "v_add_f32 v129, v129, v133\n"
    "v_mfma_f32_32x32x16_bf16 v[56:71], a[80:83], v[88:91], v[56:71]\n"
    "ds_read_b128 v[88:91], v145 offset:3072\n"
    "v_mul_f32 v128, 0xbb000000, v128\n"
    "v_mul_f32 v129, 0x3b000000, v129\n"
    "v_cvt_pk_bf16_f32 v124, v128, v128\n"
    "v_fma_f32 v129, -v128, v128, v129\n"
    "v_lshlrev_b32 v130, 16, v124\n"
    "v_max_f32 v129, 0, v129\n"
    "v_mfma_f32_32x32x16_bf16 v[56:71], a[84:87], v[92:95], v[56:71]\n"
    "v_add_f32 v129, 0x3727c5ac, v129\n"
    "v_sqrt_f32 v131, v129\n"
    "ds_read_b128 v[92:95], v145 offset:11264\n"
    "buffer_load_dwordx4 v2, s[28:31], s69 offen offset:1024 sc1 lds\n"
    "v_sub_f32 v130, v128, v130\n"
    "v_cvt_pk_bf16_f32 v125, v130, v131\n"
    "v_mfma_f32_32x32x16_bf16 v[56:71], a[88:91], v[96:99], v[56:71]\n"
    "v_and_b32 v132, 0xffff0000, v125\n"
    "v_sub_f32 v132, v131, v132\n"
    "ds_read_b128 v[96:99], v145 offset:19456\n"
    "v_cvt_pk_bf16_f32 v126, v132, 0\n"
    "s_add_u32 m0, s47, 0x20000\n"
    "v_rcp_f32 v20, v129\n"
    "buffer_load_dwordx4 v2, s[28:31], s68 offen offset:2048 sc1 lds\n"
    "s_waitcnt vmcnt(8)\n"
    "ds_read_b64_tr_b16 v[208:209], v141 offset:49152\n"
    "ds_read_b64_tr_b16 v[210:211], v141 offset:50176\n"
    "ds_read_b64_tr_b16 v[212:213], v142 offset:49152\n"
    "ds_read_b64_tr_b16 v[214:215], v142 offset:50176\n"
    "ds_read_b64_tr_b16 v[216:217], v141 offset:51200\n"
    "ds_read_b64_tr_b16 v[218:219], v141 offset:52224\n"
    "ds_read_b64_tr_b16 v[220:221], v142 offset:51200\n"
    "v_mfma_f32_32x32x16_bf16 v[40:55], v[120:123], v[124:127], v[40:55]\n"
    "ds_read_b64_tr_b16 v[222:223], v142 offset:52224\n"
    "buffer_load_dwordx4 v2, s[28:31], s68 offen offset:3072 sc1 lds\n"
    "ds_read_b64_tr_b16 v[224:225], v141 offset:53248\n"
    "ds_read_b64_tr_b16 v[226:227], v141 offset:54272\n"
    "v_mfma_f32_32x32x16_bf16 v[56:71], v[116:119], v[124:127], v[56:71]\n"
    "ds_read_b64_tr_b16 v[228:229], v142 offset:53248\n"
    "ds_read_b64_tr_b16 v[230:231], v142 offset:54272\n"
    "s_waitcnt lgkmcnt(12)\n"
    "v_mfma_f32_32x32x16_bf16 v[56:71], a[92:95], v[100:103], v[56:71]\n"
    "s_add_u32 m0, s47, 0x21000\n"
    "ds_read_b128 v[100:103], v145 offset:27648\n"
    "buffer_load_dwordx4 v2, s[28:31], s69 offen offset:2048 sc1 lds\n"
    "v_mfma_f32_32x32x16_bf16 v[56:71], a[96:99], v[72:75], v[56:71]\n"
    "ds_write_b128 v146, v[48:51]\n"
    "buffer_load_dwordx4 v2, s[28:31], s69 offen offset:3072 sc1 lds\n"
    "ds_write_b128 v146, v[52:55] offset:1024\n"
    "v_mfma_f32_32x32x16_bf16 v[56:71], a[100:103], v[76:79], v[56:71]\n"
    "s_waitcnt lgkmcnt(1)\n"
    "buffer_load_dwordx4 v[128:131], v1, s[28:31], s61 offen sc1\n"
    "v_mfma_f32_32x32x16_bf16 v[56:71], a[104:107], v[80:83], v[56:71]\n"
    "v_mfma_f32_32x32x16_bf16 v[56:71], a[108:111], v[84:87], v[56:71]\n"
    "s_waitcnt lgkmcnt(0)\n"
    "s_barrier\n"
    "buffer_load_dwordx4 v[132:135], v1, s[28:31], s61 offen offset:1024 sc1\n"
    "v_mfma_f32_32x32x16_bf16 v[56:71], a[112:115], v[88:91], v[56:71]\n"
    "ds_read_b128 v[48:51], v147\n"
    "ds_read_b128 v[52:55], v147 offset:1024\n"
    "s_add_u32 m0, s46, 0x0\n"
    "v_mfma_f32_32x32x16_bf16 v[56:71], a[116:119], v[92:95], v[56:71]\n"
    "buffer_load_dwordx4 v30, s[24:27], s57 offen offset:3072 lds\n"
    "v_mfma_f32_32x32x16_bf16 v[56:71], a[120:123], v[96:99], v[56:71]\n"
    "v_mfma_f32_32x32x16_bf16 v[56:71], a[124:127], v[100:103], v[56:71]\n"
    "s_waitcnt lgkmcnt(1)\n"
    "v_add_f32 v40, v40, v48\n"
    "v_add_f32 v41, v41, v49\n"
    "s_add_u32 m0, s46, 0x1000\n"
    "v_add_f32 v42, v42, v50\n"
    "buffer_load_dwordx4 v7, s[24:27], s57 offen lds\n"
    "v_add_f32 v43, v43, v51\n"
    "v_max_f32 v40, 0, v40\n"
    "v_max_f32 v41, 0, v41\n"
    "s_waitcnt lgkmcnt(0)\n"
    "v_add_f32 v44, v44, v52\n"
    "s_nop 0\n"
    "v_max_f32 v56, 0, v56\n"
    "v_max_f32 v57, 0, v57\n"
    "v_max_f32 v64, 0, v64\n"
    "v_max_f32 v65, 0, v65\n"
    "v_add_f32 v45, v45, v53\n"
    "v_add_f32 v46, v46, v54\n"
    "v_add_f32 v47, v47, v55\n"
    "v_mul_f32 v40, v40, v40\n"
    "v_mul_f32 v41, v41, v41\n"
    "v_max_f32 v42, 0, v42\n"
    "buffer_load_dwordx4 v32, s[24:27], s57 offen offset:1024 lds\n"
    "v_max_f32 v43, 0, v43\n"
    "v_mul_f32 v56, v56, v56\n"
    "v_mul_f32 v57, v57, v57\n"
    "v_max_f32 v58, 0, v58\n"
    "v_max_f32 v59, 0, v59\n"
    "v_mul_f32 v64, v64, v64\n"
    "v_mul_f32 v65, v65, v65\n"
    "v_max_f32 v66, 0, v66\n"
    "v_max_f32 v67, 0, v67\n"
    "v_cvt_pk_bf16_f32 v40, v40, v41\n"
    "v_mul_f32 v42, v42, v42\n"
    "v_mul_f32 v43, v43, v43\n"
    "v_max_f32 v44, 0, v44\n"
    "v_max_f32 v45, 0, v45\n"
    "v_max_f32 v46, 0, v46\n"
    "v_max_f32 v47, 0, v47\n"
    "v_cvt_pk_bf16_f32 v56, v56, v57\n"
    "s_mov_b32 s57, 0x90000\n"
    "s_add_u32 m0, s46, 0x6000\n"
    "v_mul_f32 v58, v58, v58\n"
    "buffer_load_dwordx4 v6, s[24:27], s57 offen lds\n"
    "v_mul_f32 v59, v59, v59\n"
    "v_max_f32 v60, 0, v60\n"
    "v_max_f32 v61, 0, v61\n"
    "v_max_f32 v62, 0, v62\n"
    "v_max_f32 v63, 0, v63\n"
    "v_cvt_pk_bf16_f32 v64, v64, v65\n"
    "v_mul_f32 v66, v66, v66\n"
    "v_mul_f32 v67, v67, v67\n"
    "v_max_f32 v68, 0, v68\n"
    "v_max_f32 v69, 0, v69\n"
    "v_max_f32 v70, 0, v70\n"
    "v_max_f32 v71, 0, v71\n"
    "buffer_load_dwordx4 v3, s[24:27], s57 offen offset:1024 lds\n"
    "v_cvt_pk_bf16_f32 v41, v42, v43\n"
    "v_mul_f32 v44, v44, v44\n"
    "v_mul_f32 v45, v45, v45\n"
    "v_mul_f32 v46, v46, v46\n"
    "v_mul_f32 v47, v47, v47\n"
    "v_cvt_pk_bf16_f32 v57, v58, v59\n"
    "v_mul_f32 v60, v60, v60\n"
    "v_mul_f32 v61, v61, v61\n"
    "v_mul_f32 v62, v62, v62\n"
    "v_mul_f32 v63, v63, v63\n"
    "v_cvt_pk_bf16_f32 v65, v66, v67\n"
    "v_mul_f32 v68, v68, v68\n"
    "v_mul_f32 v69, v69, v69\n"
    "v_mul_f32 v70, v70, v70\n"
    "v_mul_f32 v71, v71, v71\n"
    "v_cvt_pk_bf16_f32 v42, v44, v45\n"
    "v_cvt_pk_bf16_f32 v43, v46, v47\n"
    "v_cvt_pk_bf16_f32 v58, v60, v61\n"
    "buffer_store_dwordx4 v[40:43], v1, s[28:31], s92 offen sc1\n"
    "v_cvt_pk_bf16_f32 v59, v62, v63\n"
    "v_cvt_pk_bf16_f32 v66, v68, v69\n"
    "v_cvt_pk_bf16_f32 v67, v70, v71\n"
    "s_waitcnt vmcnt(11)\n"
    "s_barrier\n"
    "buffer_store_dwordx4 v[56:59], v1, s[28:31], s93 offen sc1\n"
    "ds_read_b128 v[88:91], v145 offset:32768\n"
    "ds_read_b128 v[92:95], v145 offset:40960\n"
    "s_waitcnt vmcnt(9)\n"
    "s_barrier\n"
    "ds_read_b128 v[96:99], v145 offset:49152\n"
    "ds_read_b128 v[100:103], v145 offset:57344\n"
    "s_add_u32 s93, s89, s58\n"
    "buffer_store_dwordx4 v[64:67], v1, s[28:31], s90 offen sc1\n"
    "ds_read_b128 v[56:59], v145 offset:34816\n"
    "s_waitcnt vmcnt(8)\n"
    "v_add_f32 v128, v128, v132\n"
    "ds_read_b128 v[60:63], v145 offset:43008\n"
    "s_waitcnt lgkmcnt(5)\n"
    "v_mfma_f32_32x32x16_bf16 v[72:87], a[64:67], v[88:91], 0\n"
    "ds_read_b128 v[88:91], v144 offset:32768\n"
    "v_add_f32 v129, v129, v133\n"
    "ds_read_b128 v[64:67], v145 offset:51200\n"
    "buffer_load_dwordx4 v4, s[24:27], s57 offen offset:2048 lds\n"
    "ds_read_b128 v[68:71], v145 offset:59392\n"
    "s_waitcnt lgkmcnt(7)\n"
    "v_mfma_f32_32x32x16_bf16 v[72:87], a[68:71], v[92:95], v[72:87]\n"
    "ds_read_b128 v[92:95], v144 offset:40960\n"
    "v_add_f32 v130, v130, v134\n"
    "v_add_f32 v131, v131, v135\n"
    "s_cmp_ge_u32 s76, 3\n"
    "s_cselect_b32 s89, s91, 0\n"
    "v_permlane32_swap_b32 v128, v130\n"
    "s_waitcnt lgkmcnt(6)\n"
    "v_mfma_f32_32x32x16_bf16 v[72:87], a[72:75], v[96:99], v[72:87]\n"
    "ds_read_b128 v[96:99], v144 offset:49152\n"
    "buffer_load_dwordx4 v30, s[24:27], s57 offen offset:3072 lds\n"
    "v_permlane32_swap_b32 v129, v131\n"
    "v_mfma_f32_32x32x16_bf16 v[72:87], a[76:79], v[100:103], v[72:87]\n"
    "ds_read_b128 v[100:103], v144 offset:57344\n"
    "v_add_f32 v128, v128, v130\n"
    "v_add_f32 v129, v129, v131\n"
    "s_add_u32 s92, s93, 0x8000\n"
    "s_add_u32 s90, s93, 0x4000\n"
    "s_add_u32 s92, s92, s89\n"
    "s_waitcnt lgkmcnt(4)\n"
    "v_mfma_f32_32x32x16_bf16 v[72:87], a[0:3], v[88:91], v[72:87]\n"
    "s_add_u32 m0, s46, 0x7000\n"
    "v_permlane16_swap_b32 v128, v129\n"
    "buffer_load_dwordx4 v7, s[24:27], s57 offen lds\n"
    "s_waitcnt vmcnt(8)\n"
    "ds_read_b64_tr_b16 v[232:233], v141\n"
    "ds_read_b64_tr_b16 v[234:235], v141 offset:1024\n"
    "ds_read_b64_tr_b16 v[236:237], v142\n"
    "ds_read_b64_tr_b16 v[238:239], v142 offset:1024\n"
    "ds_read_b64_tr_b16 v[240:241], v141 offset:2048\n"
    "v_mfma_f32_32x32x16_bf16 v[40:55], a[128:131], v[88:91], 0\n"
    "ds_read_b128 v[88:91], v144 offset:33792\n"
    "v_add_f32 v128, v128, v129\n"
    "v_and_b32 v7, 0x3fffffff, v7\n"
    "ds_read_b64_tr_b16 v[242:243], v141 offset:3072\n"
    "ds_read_b64_tr_b16 v[244:245], v142 offset:2048\n"
    "s_waitcnt lgkmcnt(8)\n"
    "v_mfma_f32_32x32x16_bf16 v[72:87], a[4:7], v[92:95], v[72:87]\n"
    "ds_read_b64_tr_b16 v[246:247], v142 offset:3072\n"
    "ds_read_b64_tr_b16 v[248:249], v141 offset:4096\n"
    "buffer_load_dwordx4 v32, s[24:27], s57 offen offset:1024 lds\n"
    "ds_read_b64_tr_b16 v[250:251], v141 offset:5120\n"
    "ds_read_b64_tr_b16 v[252:253], v142 offset:4096\n"
    "ds_read_b64_tr_b16 v[254:255], v142 offset:5120\n"
    "v_mfma_f32_32x32x16_bf16 v[40:55], a[132:135], v[92:95], v[40:55]\n"
    "ds_read_b128 v[92:95], v144 offset:41984\n"
    "v_and_b32 v32, 0x3fffffff, v32\n"
    "s_mov_b32 s57, 0x0\n"
    "s_add_u32 m0, s46, 0xc000\n"
    "ds_write_b32 v148, v128\n"
    "buffer_load_dwordx4 v6, s[24:27], s57 offen lds\n"
    "v_mfma_f32_32x32x16_bf16 v[72:87], a[8:11], v[96:99], v[72:87]\n"
    "s_waitcnt lgkmcnt(0)\n"
    "s_barrier\n"
    "buffer_load_dwordx4 v3, s[24:27], s57 offen offset:1024 lds\n"
    "ds_read2_b32 v[128:129], v149 offset0:0 offset1:32\n"
    "ds_read2_b32 v[130:131], v149 offset0:64 offset1:96\n"
    "v_mfma_f32_32x32x16_bf16 v[40:55], a[136:139], v[96:99], v[40:55]\n"
    "ds_read_b128 v[96:99], v144 offset:50176\n"
    "ds_read2_b32 v[132:133], v149 offset0:128 offset1:160\n"
    "v_mfma_f32_32x32x16_bf16 v[72:87], a[12:15], v[100:103], v[72:87]\n"
    "ds_read2_b32 v[134:135], v149 offset0:192 offset1:224\n"
    "buffer_load_dwordx4 v4, s[24:27], s57 offen offset:2048 lds\n"
    "v_mfma_f32_32x32x16_bf16 v[40:55], a[140:143], v[100:103], v[40:55]\n"
    "ds_read_b128 v[100:103], v144 offset:58368\n"
    "v_mfma_f32_32x32x16_bf16 v[72:87], a[16:19], v[88:91], v[72:87]\n"
    "s_waitcnt lgkmcnt(4)\n"
    "v_add_f32 v128, v128, v130\n"
    "v_add_f32 v129, v129, v131\n"
    "s_waitcnt vmcnt(7)\n"
    "buffer_store_dword v27, v31, s[28:31], s3 offen offset:640 sc1\n"
    "v_mfma_f32_32x32x16_bf16 v[40:55], a[144:147], v[88:91], v[40:55]\n"
    "ds_read_b128 v[88:91], v144 offset:34816\n"
    "v_mov_b32 v28, 0\n"
    "s_waitcnt lgkmcnt(2)\n"
    "v_add_f32 v132, v132, v134\n"
    "v_add_f32 v133, v133, v135\n"
    "v_add_f32 v128, v128, v132\n"
    "v_mfma_f32_32x32x16_bf16 v[72:87], a[20:23], v[92:95], v[72:87]\n"
    "v_add_f32 v129, v129, v133\n"
    "v_mul_f32 v128, 0xbb000000, v128\n"
    "buffer_load_dwordx4 v30, s[24:27], s57 offen offset:3072 lds\n"
    "v_mul_f32 v129, 0x3b000000, v129\n"
    "v_mfma_f32_32x32x16_bf16 v[40:55], a[148:151], v[92:95], v[40:55]\n"
    "ds_read_b128 v[92:95], v144 offset:43008\n"
    "v_fma_f32 v129, -v128, v128, v129\n"
    "v_cvt_pk_bf16_f32 v124, v128, v128\n"
    "v_max_f32 v129, 0, v129\n"
    "v_lshlrev_b32 v130, 16, v124\n"
    "v_add_f32 v129, 0x3727c5ac, v129\n"
    "v_mfma_f32_32x32x16_bf16 v[72:87], a[24:27], v[96:99], v[72:87]\n"
    "v_sqrt_f32 v131, v129\n"
    "s_add_u32 m0, s46, 0xd000\n"
    "v_sub_f32 v130, v128, v130\n"
    "buffer_load_dwordx4 v7, s[24:27], s57 offen lds\n"
    "v_mfma_f32_32x32x16_bf16 v[40:55], a[152:155], v[96:99], v[40:55]\n"
    "ds_read_b128 v[96:99], v144 offset:51200\n"
    "v_cvt_pk_bf16_f32 v125, v130, v131\n"
    "v_rcp_f32 v21, v129\n"
    "v_and_b32 v132, 0xffff0000, v125\n"
    "v_sub_f32 v132, v131, v132\n"
    "s_waitcnt lgkmcnt(2)\n"
    "v_mfma_f32_32x32x16_bf16 v[72:87], a[28:31], v[100:103], v[72:87]\n"
    "v_cvt_pk_bf16_f32 v126, v132, 0\n"
    "buffer_load_dwordx4 v32, s[24:27], s57 offen offset:1024 lds\n"
    "v_mfma_f32_32x32x16_bf16 v[40:55], a[156:159], v[100:103], v[40:55]\n"
    "ds_read_b128 v[100:103], v144 offset:59392\n"
    "v_mfma_f32_32x32x16_bf16 v[72:87], a[32:35], v[88:91], v[72:87]\n"
    "v_mfma_f32_32x32x16_bf16 v[40:55], a[160:163], v[88:91], v[40:55]\n"
    "s_mov_b32 s57, 0x30000\n"
    "s_add_u32 m0, s46, 0x0\n"
    "ds_read_b128 v[88:91], v144 offset:35840\n"
    "buffer_load_dwordx4 v6, s[24:27], s57 offen lds\n"
    "s_waitcnt vmcnt(8)\n"
    "ds_read_b64_tr_b16 a[192:193], v141 offset:24576\n"
    "ds_read_b64_tr_b16 a[194:195], v141 offset:25600\n"
    "ds_read_b64_tr_b16 a[196:197], v142 offset:24576\n"
    "ds_read_b64_tr_b16 a[198:199], v142 offset:25600\n"
    "ds_read_b64_tr_b16 a[200:201], v141 offset:26624\n"
    "s_waitcnt lgkmcnt(7)\n"
    "v_mfma_f32_32x32x16_bf16 v[72:87], a[36:39], v[92:95], v[72:87]\n"
    "ds_read_b64_tr_b16 a[202:203], v141 offset:27648\n"
    "ds_read_b64_tr_b16 a[204:205], v142 offset:26624\n"
    "ds_read_b64_tr_b16 a[206:207], v142 offset:27648\n"
    "ds_read_b64_tr_b16 a[208:209], v141 offset:28672\n"
    "buffer_load_dwordx4 v3, s[24:27], s57 offen offset:1024 lds\n"
    "ds_read_b64_tr_b16 a[210:211], v141 offset:29696\n"
    "ds_read_b64_tr_b16 a[212:213], v142 offset:28672\n"
    "ds_read_b64_tr_b16 a[214:215], v142 offset:29696\n"
    "v_mfma_f32_32x32x16_bf16 v[40:55], a[164:167], v[92:95], v[40:55]\n"
    "ds_read_b128 v[92:95], v144 offset:44032\n"
    "v_mfma_f32_32x32x16_bf16 v[72:87], a[40:43], v[96:99], v[72:87]\n"
    "buffer_load_dwordx4 v4, s[24:27], s57 offen offset:2048 lds\n"
    "v_mfma_f32_32x32x16_bf16 v[40:55], a[168:171], v[96:99], v[40:55]\n"
    "ds_read_b128 v[96:99], v144 offset:52224\n"
    "s_waitcnt lgkmcnt(5)\n"
    "v_mfma_f32_32x32x16_bf16 v[72:87], a[44:47], v[100:103], v[72:87]\n"
    "s_waitcnt lgkmcnt(2)\n"
    "buffer_load_dwordx4 v30, s[24:27], s57 offen offset:3072 lds\n"
    "v_mfma_f32_32x32x16_bf16 v[40:55], a[172:175], v[100:103], v[40:55]\n"
    "s_add_u32 m0, s46, 0x1000\n"
    "ds_read_b128 v[100:103], v144 offset:60416\n"
    "buffer_load_dwordx4 v7, s[24:27], s57 offen lds\n"
    "v_mfma_f32_32x32x16_bf16 v[72:87], a[48:51], v[88:91], v[72:87]\n"
    "v_mfma_f32_32x32x16_bf16 v[40:55], a[176:179], v[88:91], v[40:55]\n"
    "ds_read_b128 v[88:91], v145 offset:33792\n"
    "s_waitcnt lgkmcnt(2)\n"
    "v_mfma_f32_32x32x16_bf16 v[72:87], a[52:55], v[92:95], v[72:87]\n"
    "buffer_load_dwordx4 v32, s[24:27], s57 offen offset:1024 lds\n"
    "v_mfma_f32_32x32x16_bf16 v[40:55], a[180:183], v[92:95], v[40:55]\n"
    "ds_read_b128 v[92:95], v145 offset:41984\n"
    "s_mov_b32 s57, 0x60000\n"
    "s_add_u32 m0, s46, 0x6000\n"
    "v_mfma_f32_32x32x16_bf16 v[72:87], a[56:59], v[96:99], v[72:87]\n"
    "buffer_load_dwordx4 v6, s[24:27], s57 offen lds\n"
    "v_mfma_f32_32x32x16_bf16 v[40:55], a[184:187], v[96:99], v[40:55]\n"
    "ds_read_b128 v[96:99], v145 offset:50176\n"
    "s_waitcnt lgkmcnt(2)\n"
    "v_mfma_f32_32x32x16_bf16 v[72:87], a[60:63], v[100:103], v[72:87]\n"
    "buffer_atomic_or v28, v33, s[28:31], s4 offen offset:640 sc0 sc1\n"
    "s_waitcnt vmcnt(8)\n"
    "ds_read_b64_tr_b16 a[216:217], v141 offset:49152\n"
    "ds_read_b64_tr_b16 a[218:219], v141 offset:50176\n"
    "ds_read_b64_tr_b16 a[220:221], v142 offset:49152\n"
    "ds_read_b64_tr_b16 a[222:223], v142 offset:50176\n"
    "ds_read_b64_tr_b16 a[224:225], v141 offset:51200\n"
    "v_mfma_f32_32x32x16_bf16 v[40:55], a[188:191], v[100:103], v[40:55]\n"
    "ds_read_b128 v[100:103], v145 offset:58368\n"
    "ds_read_b64_tr_b16 a[226:227], v141 offset:52224\n"
    "ds_read_b64_tr_b16 a[228:229], v142 offset:51200\n"
    "v_mfma_f32_32x32x16_bf16 v[72:87], a[80:83], v[88:91], v[72:87]\n"
    "ds_read_b128 v[88:91], v145 offset:35840\n"
    "ds_read_b64_tr_b16 a[230:231], v142 offset:52224\n"
    "ds_read_b64_tr_b16 a[232:233], v141 offset:53248\n"
    "buffer_load_dwordx4 v3, s[24:27], s57 offen offset:1024 lds\n"
    "ds_read_b64_tr_b16 a[234:235], v141 offset:54272\n"
    "ds_read_b64_tr_b16 a[236:237], v142 offset:53248\n"
    "ds_read_b64_tr_b16 a[238:239], v142 offset:54272\n"
    "s_waitcnt lgkmcnt(13)\n"
    "v_mfma_f32_32x32x16_bf16 v[72:87], a[84:87], v[92:95], v[72:87]\n"
    "ds_read_b128 v[92:95], v145 offset:44032\n"
    "v_mfma_f32_32x32x16_bf16 v[72:87], a[88:91], v[96:99], v[72:87]\n"
    "buffer_load_dwordx4 v4, s[24:27], s57 offen offset:2048 lds\n"
    "ds_read_b128 v[96:99], v145 offset:52224\n"
    "v_mfma_f32_32x32x16_bf16 v[40:55], v[120:123], v[124:127], v[40:55]\n"
    "v_mfma_f32_32x32x16_bf16 v[72:87], v[116:119], v[124:127], v[72:87]\n"
    "buffer_load_dwordx4 v30, s[24:27], s57 offen offset:3072 lds\n"
    "s_waitcnt lgkmcnt(2)\n"
    "v_mfma_f32_32x32x16_bf16 v[72:87], a[92:95], v[100:103], v[72:87]\n"
    "ds_read_b128 v[100:103], v145 offset:60416\n"
    "s_add_u32 m0, s46, 0x7000\n"
    "v_mfma_f32_32x32x16_bf16 v[72:87], a[96:99], v[56:59], v[72:87]\n"
    "buffer_load_dwordx4 v7, s[24:27], s57 offen lds\n"
    "v_mfma_f32_32x32x16_bf16 v[72:87], a[100:103], v[60:63], v[72:87]\n"
    "s_nop 2\n"
    "ds_write_b128 v146, v[48:51]\n"
    "buffer_load_dwordx4 v32, s[24:27], s57 offen offset:1024 lds\n"
    "ds_write_b128 v146, v[52:55] offset:1024\n"
    "v_mfma_f32_32x32x16_bf16 v[72:87], a[104:107], v[64:67], v[72:87]\n"
    "buffer_load_dwordx4 v[148:151], v24, s[8:11], s52 offen\n"
    "s_waitcnt vmcnt(8)\n"
    "ds_read_b64_tr_b16 a[240:241], v141\n"
    "ds_read_b64_tr_b16 a[242:243], v141 offset:1024\n"
    "v_mfma_f32_32x32x16_bf16 v[72:87], a[108:111], v[68:71], v[72:87]\n"
    "ds_read_b64_tr_b16 a[244:245], v142\n"
    "ds_read_b64_tr_b16 a[246:247], v142 offset:1024\n"
    "ds_read_b64_tr_b16 a[248:249], v141 offset:2048\n"
    "ds_read_b64_tr_b16 a[250:251], v141 offset:3072\n"
    "ds_read_b64_tr_b16 a[252:253], v142 offset:2048\n"
    "ds_read_b64_tr_b16 a[254:255], v142 offset:3072\n"
    "v_mfma_f32_32x32x16_bf16 v[72:87], a[112:115], v[88:91], v[72:87]\n"
    "ds_read_b64_tr_b16 v[104:105], v141 offset:4096\n"
    "ds_read_b64_tr_b16 v[106:107], v141 offset:5120\n"
    "s_waitcnt lgkmcnt(10)\n"
    "s_barrier\n"
    "ds_read_b128 v[48:51], v147\n"
    "ds_read_b128 v[52:55], v147 offset:1024\n"
    "v_mfma_f32_32x32x16_bf16 v[72:87], a[116:119], v[92:95], v[72:87]\n"
    "ds_read_b64_tr_b16 v[108:109], v142 offset:4096\n"
    "s_waitcnt vmcnt(6)\n"
    "v_cmp_eq_u32 vcc, v28, v27\n"
    "s_nop 0\n"
    "s_or_b64 vcc, vcc, s[98:99]\n"
    "s_cmp_eq_u64 vcc, -1\n"
    "s_cbranch_scc0 L_spin_da_%=\n"
    "L_ok_da_%=:\n"
    "v_mfma_f32_32x32x16_bf16 v[72:87], a[120:123], v[96:99], v[72:87]\n"
    "buffer_load_dwordx4 a[0:3], v1, s[28:31], s41 offen sc1\n"
    "ds_read_b64_tr_b16 v[110:111], v142 offset:5120\n"
    "v_lshlrev_b32 v147, 2, v36\n"
    "v_mfma_f32_32x32x16_bf16 v[72:87], a[124:127], v[100:103], v[72:87]\n"
    "buffer_load_dwordx4 a[4:7], v1, s[28:31], s43 offen sc1\n"
    "v_and_b32 v36, 15, v34\n"
    "s_waitcnt lgkmcnt(3)\n"
    "v_add_f32 v40, v40, v48\n"
    "v_add_f32 v41, v41, v49\n"
    "v_add_f32 v42, v42, v50\n"
    "v_add_f32 v43, v43, v51\n"
    "v_max_f32 v40, 0, v40\n"
    "v_max_f32 v41, 0, v41\n"
    "s_waitcnt lgkmcnt(2)\n"
    "v_add_f32 v44, v44, v52\n"
    "buffer_load_dwordx4 a[8:11], v39, s[28:31], s56 offen sc1\n"
    "v_max_f32 v72, 0, v72\n"
    "v_max_f32 v73, 0, v73\n"
    "v_max_f32 v80, 0, v80\n"
    "v_max_f32 v81, 0, v81\n"
    "v_add_f32 v45, v45, v53\n"
    "v_add_f32 v46, v46, v54\n"
    "v_add_f32 v47, v47, v55\n"
    "v_mul_f32 v40, v40, v40\n"
    "v_mul_f32 v41, v41, v41\n"
    "v_max_f32 v42, 0, v42\n"
    "v_max_f32 v43, 0, v43\n"
    "v_pk_mul_f32 v[72:73], v[72:73], v[72:73]\n"
    "v_max_f32 v74, 0, v74\n"
    "buffer_load_dwordx4 a[12:15], v1, s[28:31], s41 offen offset:1024 sc1\n"
    "v_max_f32 v75, 0, v75\n"
    "v_pk_mul_f32 v[80:81], v[80:81], v[80:81]\n"
    "v_max_f32 v82, 0, v82\n"
    "v_max_f32 v83, 0, v83\n"
    "v_cvt_pk_bf16_f32 v40, v40, v41\n"
    "v_mul_f32 v42, v42, v42\n"
    "v_mul_f32 v43, v43, v43\n"
    "v_max_f32 v44, 0, v44\n"
    "v_max_f32 v45, 0, v45\n"
    "v_max_f32 v46, 0, v46\n"
    "v_max_f32 v47, 0, v47\n"
    "v_cvt_pk_bf16_f32 v72, v72, v73\n"
    "v_pk_mul_f32 v[74:75], v[74:75], v[74:75]\n"
    "v_max_f32 v76, 0, v76\n"
    "v_max_f32 v77, 0, v77\n"
    "v_max_f32 v78, 0, v78\n"
    "v_max_f32 v79, 0, v79\n"
    "buffer_load_dwordx4 a[16:19], v1, s[28:31], s43 offen offset:1024 sc1\n"
    "v_cvt_pk_bf16_f32 v80, v80, v81\n"
    "v_pk_mul_f32 v[82:83], v[82:83], v[82:83]\n"
    "v_max_f32 v84, 0, v84\n"
    "v_max_f32 v85, 0, v85\n"
    "v_max_f32 v86, 0, v86\n"
    "v_max_f32 v87, 0, v87\n"
    "v_cvt_pk_bf16_f32 v41, v42, v43\n"
    "v_mul_f32 v44, v44, v44\n"
    "v_mul_f32 v45, v45, v45\n"
    "v_mul_f32 v46, v46, v46\n"
    "v_mul_f32 v47, v47, v47\n"
    "v_cvt_pk_bf16_f32 v73, v74, v75\n"
    "v_pk_mul_f32 v[76:77], v[76:77], v[76:77]\n"
    "buffer_load_dwordx4 a[20:23], v39, s[28:31], s56 offen offset:1024 sc1\n"
    "v_pk_mul_f32 v[78:79], v[78:79], v[78:79]\n"
    "v_cvt_pk_bf16_f32 v81, v82, v83\n"
    "v_pk_mul_f32 v[84:85], v[84:85], v[84:85]\n"
    "v_pk_mul_f32 v[86:87], v[86:87], v[86:87]\n"
    "v_cvt_pk_bf16_f32 v42, v44, v45\n"
    "v_cvt_pk_bf16_f32 v43, v46, v47\n"
    "v_cvt_pk_bf16_f32 v74, v76, v77\n"
    "v_cvt_pk_bf16_f32 v75, v78, v79\n"
    "v_cvt_pk_bf16_f32 v82, v84, v85\n"
    "v_cvt_pk_bf16_f32 v83, v86, v87\n"
    "v_lshl_add_u32 v143, v36, 7, v143\n"
    "v_bfe_u32 v36, v34, 4, 1\n"
    "s_waitcnt vmcnt(7)\n"
    "ds_read_b64_tr_b16 v[112:113], v141 offset:24576\n"
    "ds_read_b64_tr_b16 v[114:115], v141 offset:25600\n"
    "buffer_store_dwordx4 v[40:43], v1, s[28:31], s92 offen sc1\n"
    "ds_read_b64_tr_b16 v[116:117], v142 offset:24576\n"
    "ds_read_b64_tr_b16 v[118:119], v142 offset:25600\n"
    "ds_read_b64_tr_b16 v[120:121], v141 offset:26624\n"
    "ds_read_b64_tr_b16 v[122:123], v141 offset:27648\n"
    "ds_read_b64_tr_b16 v[124:125], v142 offset:26624\n"
    "v_lshl_add_u32 v143, v36, 3, v143\n"
    "ds_read_b64_tr_b16 v[126:127], v142 offset:27648\n"
    "ds_read_b64_tr_b16 v[128:129], v141 offset:28672\n"
    "v_lshrrev_b32 v36, 3, v34\n"
    "ds_read_b64_tr_b16 v[130:131], v141 offset:29696\n"
    "buffer_store_dwordx4 v[72:75], v1, s[28:31], s93 offen sc1\n"
    "ds_read_b64_tr_b16 v[132:133], v142 offset:28672\n"
    "ds_read_b64_tr_b16 v[134:135], v142 offset:29696\n"
    "v_xor_b32 v140, v37, v36\n"
    "v_xor_b32 v144, v38, v36\n"
    "v_lshlrev_b32 v140, 4, v140\n"
    "v_lshlrev_b32 v144, 4, v144\n"
    "v_lshl_add_u32 v140, v36, 8, v140\n"
    "v_lshl_add_u32 v144, v36, 7, v144\n"
    "v_add_u32 v140, s53, v140\n"
    "s_lshl_b32 s53, s75, 16\n"
    "v_xor_b32 v146, 16, v140\n"
    "s_add_u32 s53, s53, s47\n"
    "v_add_u32 v144, s86, v144\n"
    "s_waitcnt vmcnt(8)\n"
    "v_lshlrev_b32 v152, 16, v148\n"
    "buffer_store_dwordx4 v[80:83], v1, s[28:31], s90 offen sc1\n"
    "s_waitcnt vmcnt(8)\n"
    "v_mfma_f32_32x32x16_bf16 v[40:55], v[160:163], a[0:3], 0\n"
    "v_and_b32 v153, 0xffff0000, v148\n"
    "v_lshlrev_b32 v154, 16, v149\n"
    "v_and_b32 v155, 0xffff0000, v149\n"
    "s_waitcnt lgkmcnt(0)\n"
    "s_barrier\n"
    "buffer_load_dwordx4 a[24:27], v1, s[28:31], s41 offen offset:2048 sc1\n"
    "ds_write_b64 v143, v[8:9]\n"
    "v_mfma_f32_32x32x16_bf16 v[56:71], v[164:167], a[0:3], 0\n"
    "ds_write_b64 v143, v[10:11] offset:2048\n"
    "v_lshlrev_b32 v156, 16, v150\n"
    "v_and_b32 v157, 0xffff0000, v150\n"
    "ds_write_b64 v143, v[12:13] offset:4096\n"
    "buffer_load_dwordx4 a[28:31], v1, s[28:31], s43 offen offset:2048 sc1\n"
    "ds_write_b64 v143, v[14:15] offset:6144\n"
    "s_waitcnt vmcnt(9)\n"
    "v_mfma_f32_32x32x16_bf16 v[40:55], v[168:171], a[4:7], v[40:55]\n"
    "ds_write_b32 v147, v20 offset:8192\n"
    "v_lshlrev_b32 v158, 16, v151\n"
    "v_and_b32 v159, 0xffff0000, v151\n"
    "ds_write_b32 v147, v21 offset:8320\n"
    "buffer_load_dwordx4 a[32:35], v39, s[28:31], s56 offen offset:2048 sc1\n"
    "v_mfma_f32_32x32x16_bf16 v[56:71], v[172:175], a[4:7], v[56:71]\n"
    "v_lshl_add_u32 v145, v36, 10, v24\n"
    "s_add_u32 s53, s53, s52\n"
    "v_add_u32 v145, s53, v145\n"
    "s_lshl_b32 s53, s74, 5\n"
    "v_lshl_add_u32 v24, v36, 2, s53\n"
    "s_waitcnt vmcnt(9)\n"
    "v_mfma_f32_32x32x16_bf16 v[40:55], v[176:179], a[8:11], v[40:55]\n"
    "buffer_load_dwordx4 a[36:39], v1, s[28:31], s41 offen offset:3072 sc1\n"
    "v_mfma_f32_32x32x16_bf16 v[56:71], v[180:183], a[8:11], v[56:71]\n"
    "s_waitcnt vmcnt(9)\n"
    "v_mfma_f32_32x32x16_bf16 v[40:55], v[184:187], a[12:15], v[40:55]\n"
    "buffer_load_dwordx4 a[40:43], v1, s[28:31], s43 offen offset:3072 sc1\n"
    "v_mfma_f32_32x32x16_bf16 v[56:71], v[188:191], a[12:15], v[56:71]\n"
    "s_waitcnt vmcnt(9)\n"
    "v_mfma_f32_32x32x16_bf16 v[40:55], v[192:195], a[16:19], v[40:55]\n"
    "v_mfma_f32_32x32x16_bf16 v[56:71], v[196:199], a[16:19], v[56:71]\n"
    "buffer_load_dwordx4 a[44:47], v39, s[28:31], s56 offen offset:3072 sc1\n"
    "s_waitcnt vmcnt(9)\n"
    "v_mfma_f32_32x32x16_bf16 v[40:55], v[200:203], a[20:23], v[40:55]\n"
    "buffer_load_dwordx4 a[48:51], v1, s[28:31], s40 offen offset:3072 sc1\n"
    "v_mfma_f32_32x32x16_bf16 v[56:71], v[204:207], a[20:23], v[56:71]\n"
    "s_waitcnt vmcnt(7)\n"
    "buffer_store_dword v27, v31, s[28:31], s3 offen offset:768 sc1\n"
    "v_mov_b32 v16, 0\n"
    "buffer_load_dwordx4 a[52:55], v1, s[28:31], s42 offen offset:3072 sc1\n"
    "s_waitcnt vmcnt(8)\n"
    "v_mfma_f32_32x32x16_bf16 v[40:55], v[208:211], a[24:27], v[40:55]\n"
    "v_mfma_f32_32x32x16_bf16 v[56:71], v[212:215], a[24:27], v[56:71]\n"
    "buffer_load_dwordx4 a[56:59], v39, s[28:31], s44 offen offset:3072 sc1\n"
    "s_waitcnt vmcnt(8)\n"
    "v_mfma_f32_32x32x16_bf16 v[40:55], v[216:219], a[28:31], v[40:55]\n"
    "v_mfma_f32_32x32x16_bf16 v[56:71], v[220:223], a[28:31], v[56:71]\n"
    "buffer_load_dwordx4 a[60:63], v1, s[28:31], s40 offen sc1\n"
    "s_waitcnt vmcnt(8)\n"
    "v_mfma_f32_32x32x16_bf16 v[40:55], v[224:227], a[32:35], v[40:55]\n"
    "v_mfma_f32_32x32x16_bf16 v[56:71], v[228:231], a[32:35], v[56:71]\n"
    "buffer_load_dwordx4 a[128:131], v1, s[28:31], s42 offen sc1\n"
    "s_waitcnt vmcnt(8)\n"
    "v_mfma_f32_32x32x16_bf16 v[40:55], v[232:235], a[36:39], v[40:55]\n"
    "v_mfma_f32_32x32x16_bf16 v[56:71], v[236:239], a[36:39], v[56:71]\n"
    "buffer_load_dwordx4 a[132:135], v1, s[28:31], s44 offen sc1\n"
    "s_waitcnt vmcnt(8)\n"
    "v_mfma_f32_32x32x16_bf16 v[40:55], v[240:243], a[40:43], v[40:55]\n"
    "v_mfma_f32_32x32x16_bf16 v[56:71], v[244:247], a[40:43], v[56:71]\n"
    "buffer_load_dwordx4 a[136:139], v1, s[28:31], s40 offen offset:1024 sc1\n"
    "s_waitcnt vmcnt(8)\n"
    "v_mfma_f32_32x32x16_bf16 v[40:55], v[248:251], a[44:47], v[40:55]\n"
    "v_mfma_f32_32x32x16_bf16 v[56:71], v[252:255], a[44:47], v[56:71]\n"
    "buffer_load_dwordx4 a[140:143], v1, s[28:31], s42 offen offset:1024 sc1\n"
    "s_waitcnt vmcnt(8)\n"
    "v_mfma_f32_32x32x16_bf16 v[40:55], a[192:195], a[48:51], v[40:55]\n"
    "buffer_load_dwordx4 a[144:147], v1, s[28:31], s44 offen offset:1024 sc1\n"
    "v_mfma_f32_32x32x16_bf16 v[56:71], a[196:199], a[48:51], v[56:71]\n"
    "buffer_load_dwordx4 a[148:151], v1, s[28:31], s40 offen offset:2048 sc1\n"
    "s_waitcnt vmcnt(8)\n"
    "v_mfma_f32_32x32x16_bf16 v[40:55], a[200:203], a[52:55], v[40:55]\n"
    "v_mfma_f32_32x32x16_bf16 v[56:71], a[204:207], a[52:55], v[56:71]\n"
    "buffer_atomic_or v16, v33, s[28:31], s4 offen offset:768 sc0 sc1\n"
    "s_waitcnt vmcnt(8)\n"
    "v_mfma_f32_32x32x16_bf16 v[40:55], a[208:211], a[56:59], v[40:55]\n"
    "v_mfma_f32_32x32x16_bf16 v[56:71], a[212:215], a[56:59], v[56:71]\n"
    "buffer_load_dwordx4 a[152:155], v1, s[28:31], s42 offen offset:2048 sc1\n"
    "s_waitcnt vmcnt(8)\n"
    "v_mfma_f32_32x32x16_bf16 v[40:55], a[216:219], a[60:63], v[40:55]\n"
    "v_mfma_f32_32x32x16_bf16 v[56:71], a[220:223], a[60:63], v[56:71]\n"
    "buffer_load_dwordx4 a[156:159], v1, s[28:31], s44 offen offset:2048 sc1\n"
    "s_waitcnt vmcnt(8)\n"
    "v_mfma_f32_32x32x16_bf16 v[40:55], a[224:227], a[128:131], v[40:55]\n"
    "v_mfma_f32_32x32x16_bf16 v[56:71], a[228:231], a[128:131], v[56:71]\n"
    "s_waitcnt vmcnt(7)\n"
    "v_mfma_f32_32x32x16_bf16 v[40:55], a[232:235], a[132:135], v[40:55]\n"
    "v_mfma_f32_32x32x16_bf16 v[56:71], a[236:239], a[132:135], v[56:71]\n"
    "s_waitcnt vmcnt(6)\n"
    "v_mfma_f32_32x32x16_bf16 v[40:55], a[240:243], a[136:139], v[40:55]\n"
    "v_mfma_f32_32x32x16_bf16 v[56:71], a[244:247], a[136:139], v[56:71]\n"
    "s_waitcnt vmcnt(5)\n"
    "v_mfma_f32_32x32x16_bf16 v[40:55], a[248:251], a[140:143], v[40:55]\n"
    "v_mfma_f32_32x32x16_bf16 v[56:71], a[252:255], a[140:143], v[56:71]\n"
    "s_waitcnt vmcnt(4)\n"
    "v_mfma_f32_32x32x16_bf16 v[40:55], v[104:107], a[144:147], v[40:55]\n"
    "v_mfma_f32_32x32x16_bf16 v[56:71], v[108:111], a[144:147], v[56:71]\n"
    "s_waitcnt vmcnt(3)\n"
    "v_mfma_f32_32x32x16_bf16 v[40:55], v[112:115], a[148:151], v[40:55]\n"
    "s_waitcnt vmcnt(2)\n"
    "v_cmp_eq_u32 vcc, v16, v27\n"
    "s_nop 0\n"
    "s_or_b64 vcc, vcc, s[98:99]\n"
    "s_cmp_eq_u64 vcc, -1\n"
    "s_cbranch_scc0 L_spin_db_%=\n"
    "L_ok_db_%=:\n"
    "buffer_load_dwordx4 a[160:163], v1, s[28:31], s59 offen sc1\n"
    "buffer_load_dwordx4 a[164:167], v1, s[28:31], s71 offen sc1\n"
    "v_mfma_f32_32x32x16_bf16 v[56:71], v[116:119], a[148:151], v[56:71]\n"
    "s_waitcnt vmcnt(3)\n"
    "v_mfma_f32_32x32x16_bf16 v[40:55], v[120:123], a[152:155], v[40:55]\n"
    "buffer_load_dwordx4 a[168:171], v39, s[28:31], s73 offen sc1\n"
    "v_mfma_f32_32x32x16_bf16 v[56:71], v[124:127], a[152:155], v[56:71]\n"
    "buffer_load_dwordx4 a[172:175], v1, s[28:31], s59 offen offset:1024 sc1\n"
    "s_waitcnt vmcnt(4)\n"
    "v_mfma_f32_32x32x16_bf16 v[40:55], v[128:131], a[156:159], v[40:55]\n"
    "v_mfma_f32_32x32x16_bf16 v[56:71], v[132:135], a[156:159], v[56:71]\n"
    "buffer_load_dwordx4 a[176:179], v1, s[28:31], s71 offen offset:1024 sc1\n"
    "s_nop 9\n"
    "ds_write_b128 v136, v[40:43]\n"
    "buffer_load_dwordx4 a[180:183], v39, s[28:31], s73 offen offset:1024 sc1\n"
    "ds_write_b128 v137, v[44:47]\n"
    "buffer_load_dwordx4 a[184:187], v1, s[28:31], s59 offen offset:2048 sc1\n"
    "ds_write_b128 v138, v[48:51]\n"
    "buffer_load_dwordx4 a[188:191], v1, s[28:31], s71 offen offset:2048 sc1\n"
    "ds_write_b128 v139, v[52:55]\n"
    "ds_write_b128 v136, v[56:59] offset:128\n"
    "buffer_load_dwordx4 a[64:67], v39, s[28:31], s73 offen offset:2048 sc1\n"
    "ds_write_b128 v137, v[60:63] offset:128\n"
    "s_waitcnt vmcnt(8)\n"
    "v_mfma_f32_32x32x16_bf16 v[72:87], v[160:163], a[160:163], 0\n"
    "buffer_load_dwordx4 a[68:71], v1, s[28:31], s59 offen offset:3072 sc1\n"
    "ds_write_b128 v138, v[64:67] offset:128\n"
    "v_mfma_f32_32x32x16_bf16 v[88:103], v[164:167], a[160:163], 0\n"
    "buffer_load_dwordx4 a[72:75], v1, s[28:31], s71 offen offset:3072 sc1\n"
    "ds_write_b128 v139, v[68:71] offset:128\n"
    "s_waitcnt vmcnt(9)\n"
    "v_mfma_f32_32x32x16_bf16 v[72:87], v[168:171], a[164:167], v[72:87]\n"
    "buffer_load_dwordx4 a[76:79], v39, s[28:31], s73 offen offset:3072 sc1\n"
    "v_mfma_f32_32x32x16_bf16 v[88:103], v[172:175], a[164:167], v[88:103]\n"
    "s_waitcnt lgkmcnt(0)\n"
    "s_barrier\n"
    "ds_read_b128 v[148:151], v144\n"
    "ds_read_b128 v[40:43], v140\n"
    "s_waitcnt vmcnt(9)\n"
    "v_mfma_f32_32x32x16_bf16 v[72:87], v[176:179], a[168:171], v[72:87]\n"
    "buffer_load_dwordx4 a[80:83], v1, s[28:31], s58 offen offset:3072 sc1\n"
    "ds_read_b128 v[44:47], v146\n"
    "ds_read_b128 v[48:51], v140 offset:8192\n"
    "v_mfma_f32_32x32x16_bf16 v[88:103], v[180:183], a[168:171], v[88:103]\n"
    "ds_read_b128 v[52:55], v146 offset:8192\n"
    "ds_read_b128 v[56:59], v140 offset:16384\n"
    "s_waitcnt vmcnt(9)\n"
    "v_mfma_f32_32x32x16_bf16 v[72:87], v[184:187], a[172:175], v[72:87]\n"
    "buffer_load_dwordx4 a[84:87], v1, s[28:31], s70 offen offset:3072 sc1\n"
    "ds_read_b128 v[60:63], v146 offset:16384\n"
    "ds_read_b128 v[64:67], v140 offset:24576\n"
    "s_waitcnt lgkmcnt(7)\n"
    "v_lshlrev_b32 v8, 16, v148\n"
    "ds_read_b128 v[68:71], v146 offset:24576\n"
    "v_mfma_f32_32x32x16_bf16 v[88:103], v[188:191], a[172:175], v[88:103]\n"
    "ds_read_b32 v38, v24 offset:8192\n"
    "v_and_b32 v9, 0xffff0000, v148\n"
    "v_lshlrev_b32 v10, 16, v149\n"
    "v_and_b32 v11, 0xffff0000, v149\n"
    "v_lshlrev_b32 v12, 16, v150\n"
    "v_and_b32 v13, 0xffff0000, v150\n"
    "s_waitcnt vmcnt(9)\n"
    "v_mfma_f32_32x32x16_bf16 v[72:87], v[192:195], a[176:179], v[72:87]\n"
    "buffer_load_dwordx4 a[88:91], v39, s[28:31], s72 offen offset:3072 sc1\n"
    "v_lshlrev_b32 v14, 16, v151\n"
    "v_and_b32 v15, 0xffff0000, v151\n"
    "ds_read_b128 v[148:151], v144 offset:4096\n"
    "v_mfma_f32_32x32x16_bf16 v[88:103], v[196:199], a[176:179], v[88:103]\n"
    "s_waitcnt lgkmcnt(5)\n"
    "v_add_f32 v40, v40, v48\n"
    "v_add_f32 v41, v41, v49\n"
    "v_add_f32 v42, v42, v50\n"
    "v_add_f32 v43, v43, v51\n"
    "v_add_f32 v44, v44, v52\n"
    "s_waitcnt vmcnt(9)\n"
    "v_mfma_f32_32x32x16_bf16 v[72:87], v[200:203], a[180:183], v[72:87]\n"
    "buffer_load_dwordx4 a[92:95], v1, s[28:31], s58 offen sc1\n"
    "v_add_f32 v45, v45, v53\n"
    "v_add_f32 v46, v46, v54\n"
    "v_add_f32 v47, v47, v55\n"
    "v_mfma_f32_32x32x16_bf16 v[88:103], v[204:207], a[180:183], v[88:103]\n"
    "s_waitcnt lgkmcnt(1)\n"
    "v_add_f32 v56, v56, v64\n"
    "v_add_f32 v57, v57, v65\n"
    "v_add_f32 v58, v58, v66\n"
    "v_add_f32 v59, v59, v67\n"
    "v_add_f32 v60, v60, v68\n"
    "s_waitcnt vmcnt(9)\n"
    "v_mfma_f32_32x32x16_bf16 v[72:87], v[208:211], a[184:187], v[72:87]\n"
    "v_add_f32 v61, v61, v69\n"
    "v_add_f32 v62, v62, v70\n"
    "buffer_load_dwordx4 a[96:99], v1, s[28:31], s70 offen sc1\n"
    "v_add_f32 v63, v63, v71\n"
    "v_mfma_f32_32x32x16_bf16 v[88:103], v[212:215], a[184:187], v[88:103]\n"
    "v_add_f32 v8, v8, v152\n"
    "v_add_f32 v9, v9, v153\n"
    "v_add_f32 v10, v10, v154\n"
    "v_add_f32 v11, v11, v155\n"
    "v_add_f32 v12, v12, v156\n"
    "v_add_f32 v13, v13, v157\n"
    "s_waitcnt vmcnt(9)\n"
    "v_mfma_f32_32x32x16_bf16 v[72:87], v[216:219], a[188:191], v[72:87]\n"
    "v_add_f32 v14, v14, v158\n"
    "v_add_f32 v15, v15, v159\n"
    "v_add_f32 v40, v40, v56\n"
    "v_add_f32 v41, v41, v57\n"
    "v_add_f32 v42, v42, v58\n"
    "v_add_f32 v43, v43, v59\n"
    "v_mfma_f32_32x32x16_bf16 v[88:103], v[220:223], a[188:191], v[88:103]\n"
    "buffer_load_dwordx4 a[100:103], v1, s[28:31], s72 offen sc1\n"
    "v_add_f32 v44, v44, v60\n"
    "v_add_f32 v45, v45, v61\n"
    "s_waitcnt vmcnt(9)\n"
    "v_mfma_f32_32x32x16_bf16 v[72:87], v[224:227], a[64:67], v[72:87]\n"
    "v_add_f32 v46, v46, v62\n"
    "v_add_f32 v47, v47, v63\n"
    "v_fma_f32 v40, v40, v38, v8\n"
    "v_fma_f32 v41, v41, v38, v9\n"
    "v_fma_f32 v42, v42, v38, v10\n"
    "v_fma_f32 v43, v43, v38, v11\n"
    "v_mfma_f32_32x32x16_bf16 v[88:103], v[228:231], a[64:67], v[88:103]\n"
    "buffer_load_dwordx4 a[104:107], v1, s[28:31], s58 offen offset:1024 sc1\n"
    "v_fma_f32 v44, v44, v38, v12\n"
    "v_fma_f32 v45, v45, v38, v13\n"
    "v_fma_f32 v46, v46, v38, v14\n"
    "s_waitcnt vmcnt(9)\n"
    "v_mfma_f32_32x32x16_bf16 v[72:87], v[232:235], a[68:71], v[72:87]\n"
    "v_fma_f32 v47, v47, v38, v15\n"
    "ds_read_b32 v38, v24 offset:8320\n"
    "v_cvt_pk_bf16_f32 v56, v40, v41\n"
    "v_cvt_pk_bf16_f32 v57, v42, v43\n"
    "v_cvt_pk_bf16_f32 v58, v44, v45\n"
    "v_mfma_f32_32x32x16_bf16 v[88:103], v[236:239], a[68:71], v[88:103]\n"
    "v_cvt_pk_bf16_f32 v59, v46, v47\n"
    "buffer_load_dwordx4 a[108:111], v1, s[28:31], s70 offen offset:1024 sc1\n"
    "s_waitcnt lgkmcnt(1)\n"
    "v_lshlrev_b32 v8, 16, v148\n"
    "s_waitcnt vmcnt(9)\n"
    "v_mfma_f32_32x32x16_bf16 v[72:87], v[240:243], a[72:75], v[72:87]\n"
    "v_and_b32 v9, 0xffff0000, v148\n"
    "v_lshlrev_b32 v10, 16, v149\n"
    "v_and_b32 v11, 0xffff0000, v149\n"
    "v_lshlrev_b32 v12, 16, v150\n"
    "v_and_b32 v13, 0xffff0000, v150\n"
    "v_lshlrev_b32 v14, 16, v151\n"
    "v_mfma_f32_32x32x16_bf16 v[88:103], v[244:247], a[72:75], v[88:103]\n"
    "v_and_b32 v15, 0xffff0000, v151\n"
    "v_add_f32 v8, v8, v152\n"
    "v_add_f32 v9, v9, v153\n"
    "buffer_load_dwordx4 a[112:115], v1, s[28:31], s72 offen offset:1024 sc1\n"
    "s_waitcnt vmcnt(9)\n"
    "v_mfma_f32_32x32x16_bf16 v[72:87], v[248:251], a[76:79], v[72:87]\n"
    "v_add_f32 v10, v10, v154\n"
    "v_add_f32 v11, v11, v155\n"
    "v_add_f32 v12, v12, v156\n"
    "v_add_f32 v13, v13, v157\n"
    "v_add_f32 v14, v14, v158\n"
    "v_add_f32 v15, v15, v159\n"
    "v_mfma_f32_32x32x16_bf16 v[88:103], v[252:255], a[76:79], v[88:103]\n"
    "buffer_load_dwordx4 a[116:119], v1, s[28:31], s58 offen offset:2048 sc1\n"
    "s_waitcnt vmcnt(9)\n"
    "v_mfma_f32_32x32x16_bf16 v[72:87], a[192:195], a[80:83], v[72:87]\n"
    "v_mfma_f32_32x32x16_bf16 v[88:103], a[196:199], a[80:83], v[88:103]\n"
    "s_waitcnt vmcnt(8)\n"
    "v_mfma_f32_32x32x16_bf16 v[72:87], a[200:203], a[84:87], v[72:87]\n"
    "buffer_load_dwordx4 a[120:123], v1, s[28:31], s70 offen offset:2048 sc1\n"
    "v_mfma_f32_32x32x16_bf16 v[88:103], a[204:207], a[84:87], v[88:103]\n"
    "s_waitcnt vmcnt(8)\n"
    "v_mfma_f32_32x32x16_bf16 v[72:87], a[208:211], a[88:91], v[72:87]\n"
    "buffer_load_dwordx4 a[124:127], v1, s[28:31], s72 offen offset:2048 sc1\n"
    "v_mfma_f32_32x32x16_bf16 v[88:103], a[212:215], a[88:91], v[88:103]\n"
    "s_waitcnt vmcnt(8)\n"
    "v_mfma_f32_32x32x16_bf16 v[72:87], a[216:219], a[92:95], v[72:87]\n"
    "buffer_store_dwordx4 v[56:59], v145, s[32:35], 0 offen sc1\n"
    "v_mfma_f32_32x32x16_bf16 v[88:103], a[220:223], a[92:95], v[88:103]\n"
    "s_waitcnt vmcnt(8)\n"
    "v_mfma_f32_32x32x16_bf16 v[72:87], a[224:227], a[96:99], v[72:87]\n"
    "v_mfma_f32_32x32x16_bf16 v[88:103], a[228:231], a[96:99], v[88:103]\n"
    "s_waitcnt vmcnt(7)\n"
    "v_mfma_f32_32x32x16_bf16 v[72:87], a[232:235], a[100:103], v[72:87]\n"
    "v_mfma_f32_32x32x16_bf16 v[88:103], a[236:239], a[100:103], v[88:103]\n"
    "s_waitcnt vmcnt(6)\n"
    "v_mfma_f32_32x32x16_bf16 v[72:87], a[240:243], a[104:107], v[72:87]\n"
    "v_mfma_f32_32x32x16_bf16 v[88:103], a[244:247], a[104:107], v[88:103]\n"
    "s_waitcnt vmcnt(5)\n"
    "v_mfma_f32_32x32x16_bf16 v[72:87], a[248:251], a[108:111], v[72:87]\n"
    "v_mfma_f32_32x32x16_bf16 v[88:103], a[252:255], a[108:111], v[88:103]\n"
    "s_waitcnt vmcnt(4)\n"
    "v_mfma_f32_32x32x16_bf16 v[72:87], v[104:107], a[112:115], v[72:87]\n"
    "v_mfma_f32_32x32x16_bf16 v[88:103], v[108:111], a[112:115], v[88:103]\n"
    "s_waitcnt vmcnt(3)\n"
    "v_mfma_f32_32x32x16_bf16 v[72:87], v[112:115], a[116:119], v[72:87]\n"
    "v_mfma_f32_32x32x16_bf16 v[88:103], v[116:119], a[116:119], v[88:103]\n"
    "s_waitcnt vmcnt(2)\n"
    "v_mfma_f32_32x32x16_bf16 v[72:87], v[120:123], a[120:123], v[72:87]\n"
    "v_mfma_f32_32x32x16_bf16 v[88:103], v[124:127], a[120:123], v[88:103]\n"
    "s_waitcnt vmcnt(1)\n"
    "v_mfma_f32_32x32x16_bf16 v[72:87], v[128:131], a[124:127], v[72:87]\n"
    "v_mfma_f32_32x32x16_bf16 v[88:103], v[132:135], a[124:127], v[88:103]\n"
    "s_nop 10\n"
    "ds_write_b128 v136, v[72:75] offset:32768\n"
    "ds_write_b128 v137, v[76:79] offset:32768\n"
    "ds_write_b128 v138, v[80:83] offset:32768\n"
    "ds_write_b128 v139, v[84:87] offset:32768\n"
    "ds_write_b128 v136, v[88:91] offset:32896\n"
    "ds_write_b128 v137, v[92:95] offset:32896\n"
    "ds_write_b128 v138, v[96:99] offset:32896\n"
    "ds_write_b128 v139, v[100:103] offset:32896\n"
    "s_waitcnt lgkmcnt(0)\n"
    "s_barrier\n"
    "ds_read_b128 v[40:43], v140 offset:32768\n"
    "ds_read_b128 v[44:47], v146 offset:32768\n"
    "ds_read_b128 v[48:51], v140 offset:40960\n"
    "ds_read_b128 v[52:55], v146 offset:40960\n"
    "ds_read_b128 v[56:59], v140 offset:49152\n"
    "ds_read_b128 v[60:63], v146 offset:49152\n"
    "ds_read_b128 v[64:67], v140 offset:57344\n"
    "ds_read_b128 v[68:71], v146 offset:57344\n"
    "s_waitcnt lgkmcnt(5)\n"
    "v_pk_add_f32 v[40:41], v[40:41], v[48:49]\n"
    "v_pk_add_f32 v[42:43], v[42:43], v[50:51]\n"
    "s_waitcnt lgkmcnt(4)\n"
    "v_pk_add_f32 v[44:45], v[44:45], v[52:53]\n"
    "v_pk_add_f32 v[46:47], v[46:47], v[54:55]\n"
    "s_waitcnt lgkmcnt(1)\n"
    "v_pk_add_f32 v[56:57], v[56:57], v[64:65]\n"
    "v_pk_add_f32 v[58:59], v[58:59], v[66:67]\n"
    "v_pk_add_f32 v[40:41], v[40:41], v[56:57]\n"
    "s_waitcnt lgkmcnt(0)\n"
    "v_pk_add_f32 v[60:61], v[60:61], v[68:69]\n"
    "v_pk_add_f32 v[62:63], v[62:63], v[70:71]\n"
    "v_pk_add_f32 v[42:43], v[42:43], v[58:59]\n"
    "v_pk_add_f32 v[44:45], v[44:45], v[60:61]\n"
    "v_pk_add_f32 v[46:47], v[46:47], v[62:63]\n"
    "v_pk_fma_f32 v[40:41], v[40:41], v[38:39], v[8:9] op_sel_hi:[1,0,1]\n"
    "v_pk_fma_f32 v[42:43], v[42:43], v[38:39], v[10:11] op_sel_hi:[1,0,1]\n"
    "v_pk_fma_f32 v[44:45], v[44:45], v[38:39], v[12:13] op_sel_hi:[1,0,1]\n"
    "v_pk_fma_f32 v[46:47], v[46:47], v[38:39], v[14:15] op_sel_hi:[1,0,1]\n"
    "v_cvt_pk_bf16_f32 v56, v40, v41\n"
    "v_cvt_pk_bf16_f32 v57, v42, v43\n"
    "v_cvt_pk_bf16_f32 v58, v44, v45\n"
    "v_cvt_pk_bf16_f32 v59, v46, v47\n"
    "buffer_store_dwordx4 v[56:59], v145, s[32:35], s55 offen sc1\n"
    "s_or_b32 s88, s76, s74\n"
    "s_cmp_eq_u32 s88, 0\n"
    "s_cselect_b32 s88, 0, 0x40000000\n"
    "s_lshl_b32 s89, s75, 2\n"
    "s_add_u32 s88, s88, s89\n"
    "s_add_u32 s88, s88, 0x1000000\n"
    "s_sub_u32 s88, s88, s87\n"
    "v_lshlrev_b32 v36, 24, v34\n"
    "buffer_store_dword v27, v36, s[28:31], s88 offen sc1\n"
    "s_branch L_exit_%=\n"
    "s_waitcnt vmcnt(0) lgkmcnt(0)\n"
    "L_pa_%=:\n"
    "s_lshr_b32 s88, s56, 18\n"
    "s_xor_b32 s88, s88, %1\n"
    "s_lshl_b32 s89, %1, 3\n"
    "s_xor_b32 s88, s88, s89\n"
    "s_and_b32 s88, s88, 8\n"
    "s_lshl_b32 s88, s88, 10\n"
    "s_and_b32 s89, s89, 128\n"
    "s_add_u32 s88, s88, s89\n"
    "s_and_b32 s89, %1, 3\n"
    "s_lshl_b32 s89, s89, 14\n"
    "s_add_u32 s88, s88, s89\n"
    "s_add_u32 s88, s88, 0xf8200\n"
    "s_add_u32 s90, s56, s88\n"
    "s_mov_b32 s91, s57\n"
    "s_cmp_eq_u32 0, 0\n"
    "s_branch L_pr_%=\n"
    "L_pb_%=:\n"
    "s_lshr_b32 s88, s28, 18\n"
    "s_xor_b32 s88, s88, %1\n"
    "s_lshl_b32 s89, %1, 3\n"
    "s_xor_b32 s88, s88, s89\n"
    "s_and_b32 s88, s88, 8\n"
    "s_lshl_b32 s88, s88, 10\n"
    "s_and_b32 s89, s89, 128\n"
    "s_add_u32 s88, s88, s89\n"
    "s_and_b32 s89, %1, 3\n"
    "s_lshl_b32 s89, s89, 14\n"
    "s_add_u32 s88, s88, s89\n"
    "s_add_u32 s88, s88, 0xf8000\n"
    "s_add_u32 s90, s28, s88\n"
    "s_mov_b32 s91, s29\n"
    "s_cmp_eq_u32 0, 1\n"
    "s_waitcnt lgkmcnt(0)\n"
    "s_branch L_pr_%=\n"
    ".p2align 6\n"
    "L_pr_%=:\n"
    "s_memtime s[92:93]\n"
    "s_waitcnt lgkmcnt(0)\n"
    "s_load_dword s95, s[90:91], 0x0\n"
    "s_load_dword s95, s[90:91], 0x500\n"
    "s_load_dword s95, s[90:91], 0x800\n"
    "s_load_dword s95, s[90:91], 0xd00\n"
    "s_load_dword s95, s[90:91], 0x1000\n"
    "s_load_dword s95, s[90:91], 0x1500\n"
    "s_load_dword s95, s[90:91], 0x1800\n"
    "s_load_dword s95, s[90:91], 0x1d00\n"
    "s_cbranch_scc1 L_pad_%=\n"
    "s_waitcnt lgkmcnt(4)\n"
    "s_memtime s[90:91]\n"
    "s_waitcnt lgkmcnt(0)\n"
    "s_sub_u32 s87, s90, s92\n"
    "s_min_u32 s87, s87, 1100\n"
    "s_branch L_pp_%=\n"
    "L_pp_%=:\n"
    "v_mul_u32_u24 v40, s87, v29\n"
    "v_xor_b32 v40, v40, v26\n"
    "v_lshlrev_b32 v42, 2, v34\n"
    "s_lshl_b32 s88, %1, 3\n"
    "s_xor_b32 s88, s88, %1\n"
    "s_bfe_u32 s88, s88, 0x10003\n"
    "s_lshl_b32 s88, s88, 2\n"
    "s_and_b32 s89, %1, 1\n"
    "s_add_u32 s88, s88, s89\n"
    "s_lshr_b32 s89, %1, 3\n"
    "s_and_b32 s89, s89, 2\n"
    "s_add_u32 s88, s88, s89\n"
    "s_lshl_b32 s88, s88, 8\n"
    "s_add_u32 s88, s88, 0x1011000\n"
    "buffer_store_dword v40, v42, s[28:31], s88 offen sc1\n"
    "s_lshr_b32 s88, s28, 18\n"
    "s_xor_b32 s88, s88, %1\n"
    "s_lshl_b32 s89, %1, 3\n"
    "s_xor_b32 s88, s88, s89\n"
    "s_and_b32 s88, s88, 8\n"
    "s_lshl_b32 s88, s88, 10\n"
    "s_and_b32 s89, s89, 128\n"
    "s_add_u32 s88, s88, s89\n"
    "s_and_b32 s89, %1, 3\n"
    "s_lshl_b32 s89, s89, 14\n"
    "s_add_u32 s88, s88, s89\n"
    "s_add_u32 s88, s88, 0xf8000\n"
    "v_lshrrev_b32 v42, 3, v34\n"
    "v_and_b32 v43, 1, v42\n"
    "v_lshlrev_b32 v43, 8, v43\n"
    "v_lshl_add_u32 v43, v42, 10, v43\n"
    "v_and_b32 v42, 7, v34\n"
    "v_lshl_add_u32 v43, v42, 4, v43\n"
    "buffer_store_dwordx4 v[44:47], v43, s[28:31], s88 offen sc1\n"
    "s_branch L_pbd_%=\n"
    "s_waitcnt vmcnt(0)\n"
    "L_pspin_%=:\n"
    "s_mov_b32 s95, 4096\n"
    "L_ploop_%=:\n"
    "buffer_load_dword v28, v39, s[28:31], 0 offen sc1\n"
    "s_waitcnt vmcnt(0)\n"
    "v_xor_b32 v28, v27, v28\n"
    "v_lshrrev_b32 v29, v38, v28\n"
    "v_cmp_ne_u32 vcc, 0, v29\n"
    "s_nop 4\n"
    "s_cbranch_vccz L_pok_%=\n"
    "s_sub_u32 s95, s95, 1\n"
    "s_cmp_lg_u32 s95, 0\n"
    "s_cbranch_scc1 L_ploop_%=\n"
    "s_branch L_pok_%=\n"
    "L_spin_kv_%=:\n"
    "v_mov_b32 v28, 0\n"
    "buffer_atomic_or v28, v32, s[28:31], 0 offen sc0 sc1\n"
    "s_mov_b32 s95, 4096\n"
    "L_loop_kv_%=:\n"
    "s_waitcnt vmcnt(0)\n"
    "v_cmp_eq_u32 vcc, v28, v27\n"
    "s_nop 0\n"
    "s_or_b64 vcc, vcc, s[82:83]\n"
    "s_cmp_eq_u64 vcc, -1\n"
    "s_cbranch_scc1 L_ok_kv_%=\n"
    "v_mov_b32 v28, 0\n"
    "buffer_atomic_or v28, v32, s[28:31], 0 offen sc0 sc1\n"
    "s_sub_u32 s95, s95, 1\n"
    "s_cmp_lg_u32 s95, 0\n"
    "s_cbranch_scc1 L_loop_kv_%=\n"
    "s_branch L_ok_kv_%=\n"
    "s_waitcnt vmcnt(0)\n"
    "L_spin_oa_%=:\n"
    "v_mov_b32 v28, 0\n"
    "buffer_atomic_or v28, v33, s[28:31], s4 offen offset:128 sc0 sc1\n"
    "s_mov_b32 s95, 4096\n"
    "L_loop_oa_%=:\n"
    "s_waitcnt vmcnt(0)\n"
    "v_cmp_eq_u32 vcc, v28, v27\n"
    "s_nop 0\n"
    "s_or_b64 vcc, vcc, s[98:99]\n"
    "s_cmp_eq_u64 vcc, -1\n"
    "s_cbranch_scc1 L_ok_oa_%=\n"
    "v_mov_b32 v28, 0\n"
    "buffer_atomic_or v28, v33, s[28:31], s4 offen offset:128 sc0 sc1\n"
    "s_sub_u32 s95, s95, 1\n"
    "s_cmp_lg_u32 s95, 0\n"
    "s_cbranch_scc1 L_loop_oa_%=\n"
    "s_branch L_ok_oa_%=\n"
    "s_waitcnt vmcnt(0)\n"
    "L_spin_ob_%=:\n"
    "v_mov_b32 v16, 0\n"
    "buffer_atomic_or v16, v33, s[28:31], s4 offen offset:256 sc0 sc1\n"
    "s_mov_b32 s95, 4096\n"
    "L_loop_ob_%=:\n"
    "s_waitcnt vmcnt(0)\n"
    "v_cmp_eq_u32 vcc, v16, v27\n"
    "s_nop 0\n"
    "s_or_b64 vcc, vcc, s[98:99]\n"
    "s_cmp_eq_u64 vcc, -1\n"
    "s_cbranch_scc1 L_ok_ob_%=\n"
    "v_mov_b32 v16, 0\n"
    "buffer_atomic_or v16, v33, s[28:31], s4 offen offset:256 sc0 sc1\n"
    "s_sub_u32 s95, s95, 1\n"
    "s_cmp_lg_u32 s95, 0\n"
    "s_cbranch_scc1 L_loop_ob_%=\n"
    "s_branch L_ok_ob_%=\n"
    "s_waitcnt vmcnt(0)\n"
    "L_spin_ha_%=:\n"
    "v_mov_b32 v28, 0\n"
    "buffer_atomic_or v28, v33, s[28:31], s4 offen offset:384 sc0 sc1\n"
    "s_mov_b32 s95, 4096\n"
    "L_loop_ha_%=:\n"
    "s_waitcnt vmcnt(0)\n"
    "v_cmp_eq_u32 vcc, v28, v27\n"
    "s_nop 0\n"
    "s_or_b64 vcc, vcc, s[98:99]\n"
    "s_cmp_eq_u64 vcc, -1\n"
    "s_cbranch_scc1 L_ok_ha_%=\n"
    "v_mov_b32 v28, 0\n"
    "buffer_atomic_or v28, v33, s[28:31], s4 offen offset:384 sc0 sc1\n"
    "s_sub_u32 s95, s95, 1\n"
    "s_cmp_lg_u32 s95, 0\n"
    "s_cbranch_scc1 L_loop_ha_%=\n"
    "s_branch L_ok_ha_%=\n"
    "s_waitcnt vmcnt(0)\n"
    "L_spin_hb_%=:\n"
    "v_mov_b32 v16, 0\n"
    "buffer_atomic_or v16, v33, s[28:31], s4 offen offset:512 sc0 sc1\n"
    "s_mov_b32 s95, 4096\n"
    "L_loop_hb_%=:\n"
    "s_waitcnt vmcnt(0)\n"
    "v_cmp_eq_u32 vcc, v16, v27\n"
    "s_nop 0\n"
    "s_or_b64 vcc, vcc, s[98:99]\n"
    "s_cmp_eq_u64 vcc, -1\n"
    "s_cbranch_scc1 L_ok_hb_%=\n"
    "v_mov_b32 v16, 0\n"
    "buffer_atomic_or v16, v33, s[28:31], s4 offen offset:512 sc0 sc1\n"
    "s_sub_u32 s95, s95, 1\n"
    "s_cmp_lg_u32 s95, 0\n"
    "s_cbranch_scc1 L_loop_hb_%=\n"
    "s_branch L_ok_hb_%=\n"
    "s_waitcnt vmcnt(0)\n"
    "L_spin_da_%=:\n"
    "v_mov_b32 v28, 0\n"
    "buffer_atomic_or v28, v33, s[28:31], s4 offen offset:640 sc0 sc1\n"
    "s_mov_b32 s95, 4096\n"
    "L_loop_da_%=:\n"
    "s_waitcnt vmcnt(0)\n"
    "v_cmp_eq_u32 vcc, v28, v27\n"
    "s_nop 0\n"
    "s_or_b64 vcc, vcc, s[98:99]\n"
    "s_cmp_eq_u64 vcc, -1\n"
    "s_cbranch_scc1 L_ok_da_%=\n"
    "v_mov_b32 v28, 0\n"
    "buffer_atomic_or v28, v33, s[28:31], s4 offen offset:640 sc0 sc1\n"
    "s_sub_u32 s95, s95, 1\n"
    "s_cmp_lg_u32 s95, 0\n"
    "s_cbranch_scc1 L_loop_da_%=\n"
    "s_branch L_ok_da_%=\n"
    "s_waitcnt vmcnt(0)\n"
    "L_spin_db_%=:\n"
    "v_mov_b32 v16, 0\n"
    "buffer_atomic_or v16, v33, s[28:31], s4 offen offset:768 sc0 sc1\n"
    "s_mov_b32 s95, 4096\n"
    "L_loop_db_%=:\n"
    "s_waitcnt vmcnt(0)\n"
    "v_cmp_eq_u32 vcc, v16, v27\n"
    "s_nop 0\n"
    "s_or_b64 vcc, vcc, s[98:99]\n"
    "s_cmp_eq_u64 vcc, -1\n"
    "s_cbranch_scc1 L_ok_db_%=\n"
    "v_mov_b32 v16, 0\n"
    "buffer_atomic_or v16, v33, s[28:31], s4 offen offset:768 sc0 sc1\n"
    "s_sub_u32 s95, s95, 1\n"
    "s_cmp_lg_u32 s95, 0\n"
    "s_cbranch_scc1 L_loop_db_%=\n"
    "s_branch L_ok_db_%=\n"
    "s_waitcnt vmcnt(0)\n"
    "L_end_%=:\n"
    "L_slowa_%=:\n"
    "s_waitcnt vmcnt(0) lgkmcnt(0)\n"
    "s_nop 15\n"
    "v_accvgpr_write_b32 a0, v168\n"
    "v_accvgpr_write_b32 a1, v169\n"
    "v_accvgpr_write_b32 a2, v170\n"
    "v_accvgpr_write_b32 a3, v171\n"
    "v_accvgpr_write_b32 a4, v172\n"
    "v_accvgpr_write_b32 a5, v173\n"
    "v_accvgpr_write_b32 a6, v174\n"
    "v_accvgpr_write_b32 a7, v175\n"
    "v_accvgpr_write_b32 a8, v176\n"
    "v_accvgpr_write_b32 a9, v177\n"
    "v_accvgpr_write_b32 a10, v178\n"
    "v_accvgpr_write_b32 a11, v179\n"
    "v_accvgpr_write_b32 a12, v180\n"
    "v_accvgpr_write_b32 a13, v181\n"
    "v_accvgpr_write_b32 a14, v182\n"
    "v_accvgpr_write_b32 a15, v183\n"
    "v_accvgpr_write_b32 a16, v184\n"
    "v_accvgpr_write_b32 a17, v185\n"
    "v_accvgpr_write_b32 a18, v186\n"
    "v_accvgpr_write_b32 a19, v187\n"
    "v_accvgpr_write_b32 a20, v188\n"
    "v_accvgpr_write_b32 a21, v189\n"
    "v_accvgpr_write_b32 a22, v190\n"
    "v_accvgpr_write_b32 a23, v191\n"
    "v_accvgpr_write_b32 a24, v192\n"
    "v_accvgpr_write_b32 a25, v193\n"
    "v_accvgpr_write_b32 a26, v194\n"
    "v_accvgpr_write_b32 a27, v195\n"
    "v_accvgpr_write_b32 a28, v196\n"
    "v_accvgpr_write_b32 a29, v197\n"
    "v_accvgpr_write_b32 a30, v198\n"
    "v_accvgpr_write_b32 a31, v199\n"
    "v_accvgpr_write_b32 a32, v200\n"
    "v_accvgpr_write_b32 a33, v201\n"
    "v_accvgpr_write_b32 a34, v202\n"
    "v_accvgpr_write_b32 a35, v203\n"
    "v_accvgpr_write_b32 a36, v204\n"
    "v_accvgpr_write_b32 a37, v205\n"
    "v_accvgpr_write_b32 a38, v206\n"
    "v_accvgpr_write_b32 a39, v207\n"
    "v_accvgpr_write_b32 a40, v208\n"
    "v_accvgpr_write_b32 a41, v209\n"
    "v_accvgpr_write_b32 a42, v210\n"
    "v_accvgpr_write_b32 a43, v211\n"
    "v_accvgpr_write_b32 a44, v212\n"
    "v_accvgpr_write_b32 a45, v213\n"
    "v_accvgpr_write_b32 a46, v214\n"
    "v_accvgpr_write_b32 a47, v215\n"
    "v_accvgpr_write_b32 a48, v216\n"
    "v_accvgpr_write_b32 a49, v217\n"
    "v_accvgpr_write_b32 a50, v218\n"
    "v_accvgpr_write_b32 a51, v219\n"
    "v_accvgpr_write_b32 a52, v220\n"
    "v_accvgpr_write_b32 a53, v221\n"
    "v_accvgpr_write_b32 a54, v222\n"
    "v_accvgpr_write_b32 a55, v223\n"
    "s_nop 0\n"
    "v_mov_b32 v38, 0xf149f2ca\n"
    "v_mov_b32 v39, 0\n"
    "v_cmp_le_i32 s[88:89], 0, v36\n"
    "v_cmp_le_i32 s[90:91], 1, v36\n"
    "v_cmp_le_i32 s[48:49], 2, v36\n"
    "v_cmp_le_i32 s[50:51], 3, v36\n"
    "v_cndmask_b32 v208, v37, v39, s[88:89]\n"
    "v_cmp_le_i32 s[88:89], 8, v36\n"
    "v_cndmask_b32 v209, v37, v39, s[90:91]\n"
    "v_cmp_le_i32 s[90:91], 9, v36\n"
    "v_cndmask_b32 v210, v37, v39, s[48:49]\n"
    "v_cmp_le_i32 s[48:49], 10, v36\n"
    "v_cndmask_b32 v211, v37, v39, s[50:51]\n"
    "v_cmp_le_i32 s[50:51], 11, v36\n"
    "v_cndmask_b32 v212, v37, v39, s[88:89]\n"
    "v_cmp_le_i32 s[88:89], 16, v36\n"
    "v_cndmask_b32 v213, v37, v39, s[90:91]\n"
    "v_cmp_le_i32 s[90:91], 17, v36\n"
    "v_cndmask_b32 v214, v37, v39, s[48:49]\n"
    "v_cmp_le_i32 s[48:49], 18, v36\n"
    "v_cndmask_b32 v215, v37, v39, s[50:51]\n"
    "v_cmp_le_i32 s[50:51], 19, v36\n"
    "v_cndmask_b32 v216, v37, v39, s[88:89]\n"
    "v_cmp_le_i32 s[88:89], 24, v36\n"
    "v_cndmask_b32 v217, v37, v39, s[90:91]\n"
    "v_cmp_le_i32 s[90:91], 25, v36\n"
    "v_cndmask_b32 v218, v37, v39, s[48:49]\n"
    "v_cmp_le_i32 s[48:49], 26, v36\n"
    "v_cndmask_b32 v219, v37, v39, s[50:51]\n"
    "v_cmp_le_i32 s[50:51], 27, v36\n"
    "v_cndmask_b32 v220, v37, v39, s[88:89]\n"
    "v_cndmask_b32 v221, v37, v39, s[90:91]\n"
    "v_cndmask_b32 v222, v37, v39, s[48:49]\n"
    "v_cndmask_b32 v223, v37, v39, s[50:51]\n"
    "s_nop 1\n"
    "v_mfma_f32_32x32x16_bf16 v[168:183], v[40:43], v[64:67], v[208:223]\n"
    "s_nop 11\n"
    "v_max3_f32 v38, v38, v168, v169\n"
    "v_max3_f32 v38, v38, v170, v171\n"
    "v_max3_f32 v38, v38, v172, v173\n"
    "v_max3_f32 v38, v38, v174, v175\n"
    "v_max3_f32 v38, v38, v176, v177\n"
    "v_max3_f32 v38, v38, v178, v179\n"
    "v_max3_f32 v38, v38, v180, v181\n"
    "v_max3_f32 v38, v38, v182, v183\n"
    "s_cmp_lt_u32 s75, 1\n"
    "s_cbranch_scc1 L_sl_a0_%=\n"
    "v_mov_b32 v208, v39\n"
    "v_mov_b32 v209, v39\n"
    "v_mov_b32 v210, v39\n"
    "v_mov_b32 v211, v39\n"
    "v_mov_b32 v212, v39\n"
    "v_mov_b32 v213, v39\n"
    "v_mov_b32 v214, v39\n"
    "v_mov_b32 v215, v39\n"
    "v_mov_b32 v216, v39\n"
    "v_mov_b32 v217, v39\n"
    "v_mov_b32 v218, v39\n"
    "v_mov_b32 v219, v39\n"
    "v_mov_b32 v220, v39\n"
    "v_mov_b32 v221, v39\n"
    "v_mov_b32 v222, v39\n"
    "v_mov_b32 v223, v39\n"
    "s_nop 1\n"
    "v_mfma_f32_32x32x16_bf16 v[168:183], v[100:103], v[64:67], v[208:223]\n"
    "s_nop 11\n"
    "v_max3_f32 v38, v38, v168, v169\n"
    "v_max3_f32 v38, v38, v170, v171\n"
    "v_max3_f32 v38, v38, v172, v173\n"
    "v_max3_f32 v38, v38, v174, v175\n"
    "v_max3_f32 v38, v38, v176, v177\n"
    "v_max3_f32 v38, v38, v178, v179\n"
    "v_max3_f32 v38, v38, v180, v181\n"
    "v_max3_f32 v38, v38, v182, v183\n"
    "s_cmp_lt_u32 s75, 1\n"
    "s_cbranch_scc1 L_sl_a0_%=\n"
    "v_mfma_f32_32x32x16_bf16 v[168:183], v[96:99], v[64:67], v[208:223]\n"
    "s_nop 11\n"
    "v_max3_f32 v38, v38, v168, v169\n"
    "v_max3_f32 v38, v38, v170, v171\n"
    "v_max3_f32 v38, v38, v172, v173\n"
    "v_max3_f32 v38, v38, v174, v175\n"
    "v_max3_f32 v38, v38, v176, v177\n"
    "v_max3_f32 v38, v38, v178, v179\n"
    "v_max3_f32 v38, v38, v180, v181\n"
    "v_max3_f32 v38, v38, v182, v183\n"
    "s_cmp_lt_u32 s75, 2\n"
    "s_cbranch_scc1 L_sl_a0_%=\n"
    "v_mfma_f32_32x32x16_bf16 v[168:183], v[92:95], v[64:67], v[208:223]\n"
    "s_nop 11\n"
    "v_max3_f32 v38, v38, v168, v169\n"
    "v_max3_f32 v38, v38, v170, v171\n"
    "v_max3_f32 v38, v38, v172, v173\n"
    "v_max3_f32 v38, v38, v174, v175\n"
    "v_max3_f32 v38, v38, v176, v177\n"
    "v_max3_f32 v38, v38, v178, v179\n"
    "v_max3_f32 v38, v38, v180, v181\n"
    "v_max3_f32 v38, v38, v182, v183\n"
    "s_cmp_lt_u32 s75, 2\n"
    "s_cbranch_scc1 L_sl_a0_%=\n"
    "v_mfma_f32_32x32x16_bf16 v[168:183], v[88:91], v[64:67], v[208:223]\n"
    "s_nop 11\n"
    "v_max3_f32 v38, v38, v168, v169\n"
    "v_max3_f32 v38, v38, v170, v171\n"
    "v_max3_f32 v38, v38, v172, v173\n"
    "v_max3_f32 v38, v38, v174, v175\n"
    "v_max3_f32 v38, v38, v176, v177\n"
    "v_max3_f32 v38, v38, v178, v179\n"
    "v_max3_f32 v38, v38, v180, v181\n"
    "v_max3_f32 v38, v38, v182, v183\n"
    "s_cmp_lt_u32 s75, 3\n"
    "s_cbranch_scc1 L_sl_a0_%=\n"
    "v_mfma_f32_32x32x16_bf16 v[168:183], v[84:87], v[64:67], v[208:223]\n"
    "s_nop 11\n"
    "v_max3_f32 v38, v38, v168, v169\n"
    "v_max3_f32 v38, v38, v170, v171\n"
    "v_max3_f32 v38, v38, v172, v173\n"
    "v_max3_f32 v38, v38, v174, v175\n"
    "v_max3_f32 v38, v38, v176, v177\n"
    "v_max3_f32 v38, v38, v178, v179\n"
    "v_max3_f32 v38, v38, v180, v181\n"
    "v_max3_f32 v38, v38, v182, v183\n"
    "s_cmp_lt_u32 s75, 3\n"
    "s_cbranch_scc1 L_sl_a0_%=\n"
    "v_mfma_f32_32x32x16_bf16 v[168:183], v[80:83], v[64:67], v[208:223]\n"
    "s_nop 11\n"
    "v_max3_f32 v38, v38, v168, v169\n"
    "v_max3_f32 v38, v38, v170, v171\n"
    "v_max3_f32 v38, v38, v172, v173\n"
    "v_max3_f32 v38, v38, v174, v175\n"
    "v_max3_f32 v38, v38, v176, v177\n"
    "v_max3_f32 v38, v38, v178, v179\n"
    "v_max3_f32 v38, v38, v180, v181\n"
    "v_max3_f32 v38, v38, v182, v183\n"
    "s_cmp_lt_u32 s75, 4\n"
    "s_cbranch_scc1 L_sl_a0_%=\n"
    "v_mfma_f32_32x32x16_bf16 v[168:183], v[76:79], v[64:67], v[208:223]\n"
    "s_nop 11\n"
    "v_max3_f32 v38, v38, v168, v169\n"
    "v_max3_f32 v38, v38, v170, v171\n"
    "v_max3_f32 v38, v38, v172, v173\n"
    "v_max3_f32 v38, v38, v174, v175\n"
    "v_max3_f32 v38, v38, v176, v177\n"
    "v_max3_f32 v38, v38, v178, v179\n"
    "v_max3_f32 v38, v38, v180, v181\n"
    "v_max3_f32 v38, v38, v182, v183\n"
    "s_cmp_lt_u32 s75, 4\n"
    "s_cbranch_scc1 L_sl_a0_%=\n"
    "v_cmp_gt_i32 s[88:89], 0, v36\n"
    "v_cmp_gt_i32 s[90:91], 1, v36\n"
    "v_cmp_gt_i32 s[48:49], 2, v36\n"
    "v_cmp_gt_i32 s[50:51], 3, v36\n"
    "v_cndmask_b32 v208, v37, v39, s[88:89]\n"
    "v_cmp_gt_i32 s[88:89], 8, v36\n"
    "v_cndmask_b32 v209, v37, v39, s[90:91]\n"
    "v_cmp_gt_i32 s[90:91], 9, v36\n"
    "v_cndmask_b32 v210, v37, v39, s[48:49]\n"
    "v_cmp_gt_i32 s[48:49], 10, v36\n"
    "v_cndmask_b32 v211, v37, v39, s[50:51]\n"
    "v_cmp_gt_i32 s[50:51], 11, v36\n"
    "v_cndmask_b32 v212, v37, v39, s[88:89]\n"
    "v_cmp_gt_i32 s[88:89], 16, v36\n"
    "v_cndmask_b32 v213, v37, v39, s[90:91]\n"
    "v_cmp_gt_i32 s[90:91], 17, v36\n"
    "v_cndmask_b32 v214, v37, v39, s[48:49]\n"
    "v_cmp_gt_i32 s[48:49], 18, v36\n"
    "v_cndmask_b32 v215, v37, v39, s[50:51]\n"
    "v_cmp_gt_i32 s[50:51], 19, v36\n"
    "v_cndmask_b32 v216, v37, v39, s[88:89]\n"
    "v_cmp_gt_i32 s[88:89], 24, v36\n"
    "v_cndmask_b32 v217, v37, v39, s[90:91]\n"
    "v_cmp_gt_i32 s[90:91], 25, v36\n"
    "v_cndmask_b32 v218, v37, v39, s[48:49]\n"
    "v_cmp_gt_i32 s[48:49], 26, v36\n"
    "v_cndmask_b32 v219, v37, v39, s[50:51]\n"
    "v_cmp_gt_i32 s[50:51], 27, v36\n"
    "v_cndmask_b32 v220, v37, v39, s[88:89]\n"
    "v_cndmask_b32 v221, v37, v39, s[90:91]\n"
    "v_cndmask_b32 v222, v37, v39, s[48:49]\n"
    "v_cndmask_b32 v223, v37, v39, s[50:51]\n"
    "s_nop 1\n"
    "v_mfma_f32_32x32x16_bf16 v[168:183], v[72:75], v[64:67], v[208:223]\n"
    "s_nop 11\n"
    "v_max3_f32 v38, v38, v168, v169\n"
    "v_max3_f32 v38, v38, v170, v171\n"
    "v_max3_f32 v38, v38, v172, v173\n"
    "v_max3_f32 v38, v38, v174, v175\n"
    "v_max3_f32 v38, v38, v176, v177\n"
    "v_max3_f32 v38, v38, v178, v179\n"
    "v_max3_f32 v38, v38, v180, v181\n"
    "v_max3_f32 v38, v38, v182, v183\n"
    "L_sl_a0_%=:\n"
    "s_waitcnt vmcnt(0) lgkmcnt(0)\n"
    "s_nop 15\n"
    "v_mov_b32 v39, v38\n"
    "s_nop 1\n"
    "v_permlane32_swap_b32 v38, v39\n"
    "v_max_f32 v39, v38, v39\n"
    "v_xor_b32 v39, 0x80000000, v39\n"
    "v_cmp_le_i32 s[88:89], 0, v36\n"
    "v_cmp_le_i32 s[90:91], 1, v36\n"
    "v_cmp_le_i32 s[48:49], 2, v36\n"
    "v_cmp_le_i32 s[50:51], 3, v36\n"
    "v_cndmask_b32 v208, v37, v39, s[88:89]\n"
    "v_cmp_le_i32 s[88:89], 8, v36\n"
    "v_cndmask_b32 v209, v37, v39, s[90:91]\n"
    "v_cmp_le_i32 s[90:91], 9, v36\n"
    "v_cndmask_b32 v210, v37, v39, s[48:49]\n"
    "v_cmp_le_i32 s[48:49], 10, v36\n"
    "v_cndmask_b32 v211, v37, v39, s[50:51]\n"
    "v_cmp_le_i32 s[50:51], 11, v36\n"
    "v_cndmask_b32 v212, v37, v39, s[88:89]\n"
    "v_cmp_le_i32 s[88:89], 16, v36\n"
    "v_cndmask_b32 v213, v37, v39, s[90:91]\n"
    "v_cmp_le_i32 s[90:91], 17, v36\n"
    "v_cndmask_b32 v214, v37, v39, s[48:49]\n"
    "v_cmp_le_i32 s[48:49], 18, v36\n"
    "v_cndmask_b32 v215, v37, v39, s[50:51]\n"
    "v_cmp_le_i32 s[50:51], 19, v36\n"
    "v_cndmask_b32 v216, v37, v39, s[88:89]\n"
    "v_cmp_le_i32 s[88:89], 24, v36\n"
    "v_cndmask_b32 v217, v37, v39, s[90:91]\n"
    "v_cmp_le_i32 s[90:91], 25, v36\n"
    "v_cndmask_b32 v218, v37, v39, s[48:49]\n"
    "v_cmp_le_i32 s[48:49], 26, v36\n"
    "v_cndmask_b32 v219, v37, v39, s[50:51]\n"
    "v_cmp_le_i32 s[50:51], 27, v36\n"
    "v_cndmask_b32 v220, v37, v39, s[88:89]\n"
    "v_cndmask_b32 v221, v37, v39, s[90:91]\n"
    "v_cndmask_b32 v222, v37, v39, s[48:49]\n"
    "v_cndmask_b32 v223, v37, v39, s[50:51]\n"
    "s_nop 1\n"
    "v_mfma_f32_32x32x16_bf16 v[168:183], v[40:43], v[64:67], v[208:223]\n"
    "s_nop 11\n"
    "v_exp_f32 v168, v168\n"
    "v_exp_f32 v169, v169\n"
    "v_exp_f32 v170, v170\n"
    "v_exp_f32 v171, v171\n"
    "v_exp_f32 v172, v172\n"
    "v_exp_f32 v173, v173\n"
    "v_exp_f32 v174, v174\n"
    "v_exp_f32 v175, v175\n"
    "v_exp_f32 v176, v176\n"
    "v_exp_f32 v177, v177\n"
    "v_exp_f32 v178, v178\n"
    "v_exp_f32 v179, v179\n"
    "v_exp_f32 v180, v180\n"
    "v_exp_f32 v181, v181\n"
    "v_exp_f32 v182, v182\n"
    "v_exp_f32 v183, v183\n"
    "v_cvt_pk_bf16_f32 v200, v168, v169\n"
    "v_cvt_pk_bf16_f32 v201, v170, v171\n"
    "v_cvt_pk_bf16_f32 v202, v172, v173\n"
    "v_cvt_pk_bf16_f32 v203, v174, v175\n"
    "v_cvt_pk_bf16_f32 v204, v176, v177\n"
    "v_cvt_pk_bf16_f32 v205, v178, v179\n"
    "v_cvt_pk_bf16_f32 v206, v180, v181\n"
    "v_cvt_pk_bf16_f32 v207, v182, v183\n"
    "v_mfma_f32_32x32x16_bf16 v[224:239], v[48:51], v[200:203], 0\n"
    "s_nop 0\n"
    "v_mfma_f32_32x32x16_bf16 v[224:239], v[52:55], v[204:207], v[224:239]\n"
    "s_cmp_lt_u32 s75, 1\n"
    "s_cbranch_scc1 L_sl_a1_%=\n"
    "v_mov_b32 v208, v39\n"
    "v_mov_b32 v209, v39\n"
    "v_mov_b32 v210, v39\n"
    "v_mov_b32 v211, v39\n"
    "v_mov_b32 v212, v39\n"
    "v_mov_b32 v213, v39\n"
    "v_mov_b32 v214, v39\n"
    "v_mov_b32 v215, v39\n"
    "v_mov_b32 v216, v39\n"
    "v_mov_b32 v217, v39\n"
    "v_mov_b32 v218, v39\n"
    "v_mov_b32 v219, v39\n"
    "v_mov_b32 v220, v39\n"
    "v_mov_b32 v221, v39\n"
    "v_mov_b32 v222, v39\n"
    "v_mov_b32 v223, v39\n"
    "s_nop 1\n"
    "v_mfma_f32_32x32x16_bf16 v[168:183], v[100:103], v[64:67], v[208:223]\n"
    "s_nop 11\n"
    "v_exp_f32 v168, v168\n"
    "v_exp_f32 v169, v169\n"
    "v_exp_f32 v170, v170\n"
    "v_exp_f32 v171, v171\n"
    "v_exp_f32 v172, v172\n"
    "v_exp_f32 v173, v173\n"
    "v_exp_f32 v174, v174\n"
    "v_exp_f32 v175, v175\n"
    "v_exp_f32 v176, v176\n"
    "v_exp_f32 v177, v177\n"
    "v_exp_f32 v178, v178\n"
    "v_exp_f32 v179, v179\n"
    "v_exp_f32 v180, v180\n"
    "v_exp_f32 v181, v181\n"
    "v_exp_f32 v182, v182\n"
    "v_exp_f32 v183, v183\n"
    "v_cvt_pk_bf16_f32 v200, v168, v169\n"
    "v_cvt_pk_bf16_f32 v201, v170, v171\n"
    "v_cvt_pk_bf16_f32 v202, v172, v173\n"
    "v_cvt_pk_bf16_f32 v203, v174, v175\n"
    "v_cvt_pk_bf16_f32 v204, v176, v177\n"
    "v_cvt_pk_bf16_f32 v205, v178, v179\n"
    "v_cvt_pk_bf16_f32 v206, v180, v181\n"
    "v_cvt_pk_bf16_f32 v207, v182, v183\n"
    "v_mfma_f32_32x32x16_bf16 v[224:239], v[160:163], v[200:203], v[224:239]\n"
    "s_nop 0\n"
    "v_mfma_f32_32x32x16_bf16 v[224:239], v[164:167], v[204:207], v[224:239]\n"
    "s_cmp_lt_u32 s75, 1\n"
    "s_cbranch_scc1 L_sl_a1_%=\n"
    "v_mfma_f32_32x32x16_bf16 v[168:183], v[96:99], v[64:67], v[208:223]\n"
    "s_nop 11\n"
    "v_exp_f32 v168, v168\n"
    "v_exp_f32 v169, v169\n"
    "v_exp_f32 v170, v170\n"
    "v_exp_f32 v171, v171\n"
    "v_exp_f32 v172, v172\n"
    "v_exp_f32 v173, v173\n"
    "v_exp_f32 v174, v174\n"
    "v_exp_f32 v175, v175\n"
    "v_exp_f32 v176, v176\n"
    "v_exp_f32 v177, v177\n"
    "v_exp_f32 v178, v178\n"
    "v_exp_f32 v179, v179\n"
    "v_exp_f32 v180, v180\n"
    "v_exp_f32 v181, v181\n"
    "v_exp_f32 v182, v182\n"
    "v_exp_f32 v183, v183\n"
    "v_cvt_pk_bf16_f32 v200, v168, v169\n"
    "v_cvt_pk_bf16_f32 v201, v170, v171\n"
    "v_cvt_pk_bf16_f32 v202, v172, v173\n"
    "v_cvt_pk_bf16_f32 v203, v174, v175\n"
    "v_cvt_pk_bf16_f32 v204, v176, v177\n"
    "v_cvt_pk_bf16_f32 v205, v178, v179\n"
    "v_cvt_pk_bf16_f32 v206, v180, v181\n"
    "v_cvt_pk_bf16_f32 v207, v182, v183\n"
    "v_mfma_f32_32x32x16_bf16 v[224:239], v[152:155], v[200:203], v[224:239]\n"
    "s_nop 0\n"
    "v_mfma_f32_32x32x16_bf16 v[224:239], v[156:159], v[204:207], v[224:239]\n"
    "s_cmp_lt_u32 s75, 2\n"
    "s_cbranch_scc1 L_sl_a1_%=\n"
    "v_mfma_f32_32x32x16_bf16 v[168:183], v[92:95], v[64:67], v[208:223]\n"
    "s_nop 11\n"
    "v_exp_f32 v168, v168\n"
    "v_exp_f32 v169, v169\n"
    "v_exp_f32 v170, v170\n"
    "v_exp_f32 v171, v171\n"
    "v_exp_f32 v172, v172\n"
    "v_exp_f32 v173, v173\n"
    "v_exp_f32 v174, v174\n"
    "v_exp_f32 v175, v175\n"
    "v_exp_f32 v176, v176\n"
    "v_exp_f32 v177, v177\n"
    "v_exp_f32 v178, v178\n"
    "v_exp_f32 v179, v179\n"
    "v_exp_f32 v180, v180\n"
    "v_exp_f32 v181, v181\n"
    "v_exp_f32 v182, v182\n"
    "v_exp_f32 v183, v183\n"
    "v_cvt_pk_bf16_f32 v200, v168, v169\n"
    "v_cvt_pk_bf16_f32 v201, v170, v171\n"
    "v_cvt_pk_bf16_f32 v202, v172, v173\n"
    "v_cvt_pk_bf16_f32 v203, v174, v175\n"
    "v_cvt_pk_bf16_f32 v204, v176, v177\n"
    "v_cvt_pk_bf16_f32 v205, v178, v179\n"
    "v_cvt_pk_bf16_f32 v206, v180, v181\n"
    "v_cvt_pk_bf16_f32 v207, v182, v183\n"
    "v_mfma_f32_32x32x16_bf16 v[224:239], v[144:147], v[200:203], v[224:239]\n"
    "s_nop 0\n"
    "v_mfma_f32_32x32x16_bf16 v[224:239], v[148:151], v[204:207], v[224:239]\n"
    "s_cmp_lt_u32 s75, 2\n"
    "s_cbranch_scc1 L_sl_a1_%=\n"
    "v_mfma_f32_32x32x16_bf16 v[168:183], v[88:91], v[64:67], v[208:223]\n"
    "s_nop 11\n"
    "v_exp_f32 v168, v168\n"
    "v_exp_f32 v169, v169\n"
    "v_exp_f32 v170, v170\n"
    "v_exp_f32 v171, v171\n"
    "v_exp_f32 v172, v172\n"
    "v_exp_f32 v173, v173\n"
    "v_exp_f32 v174, v174\n"
    "v_exp_f32 v175, v175\n"
    "v_exp_f32 v176, v176\n"
    "v_exp_f32 v177, v177\n"
    "v_exp_f32 v178, v178\n"
    "v_exp_f32 v179, v179\n"
    "v_exp_f32 v180, v180\n"
    "v_exp_f32 v181, v181\n"
    "v_exp_f32 v182, v182\n"
    "v_exp_f32 v183, v183\n"
    "v_cvt_pk_bf16_f32 v200, v168, v169\n"
    "v_cvt_pk_bf16_f32 v201, v170, v171\n"
    "v_cvt_pk_bf16_f32 v202, v172, v173\n"
    "v_cvt_pk_bf16_f32 v203, v174, v175\n"
    "v_cvt_pk_bf16_f32 v204, v176, v177\n"
    "v_cvt_pk_bf16_f32 v205, v178, v179\n"
    "v_cvt_pk_bf16_f32 v206, v180, v181\n"
    "v_cvt_pk_bf16_f32 v207, v182, v183\n"
    "v_mfma_f32_32x32x16_bf16 v[224:239], v[136:139], v[200:203], v[224:239]\n"
    "s_nop 0\n"
    "v_mfma_f32_32x32x16_bf16 v[224:239], v[140:143], v[204:207], v[224:239]\n"
    "s_cmp_lt_u32 s75, 3\n"
    "s_cbranch_scc1 L_sl_a1_%=\n"
    "v_mfma_f32_32x32x16_bf16 v[168:183], v[84:87], v[64:67], v[208:223]\n"
    "s_nop 11\n"
    "v_exp_f32 v168, v168\n"
    "v_exp_f32 v169, v169\n"
    "v_exp_f32 v170, v170\n"
    "v_exp_f32 v171, v171\n"
    "v_exp_f32 v172, v172\n"
    "v_exp_f32 v173, v173\n"
    "v_exp_f32 v174, v174\n"
    "v_exp_f32 v175, v175\n"
    "v_exp_f32 v176, v176\n"
    "v_exp_f32 v177, v177\n"
    "v_exp_f32 v178, v178\n"
    "v_exp_f32 v179, v179\n"
    "v_exp_f32 v180, v180\n"
    "v_exp_f32 v181, v181\n"
    "v_exp_f32 v182, v182\n"
    "v_exp_f32 v183, v183\n"
    "v_cvt_pk_bf16_f32 v200, v168, v169\n"
    "v_cvt_pk_bf16_f32 v201, v170, v171\n"
    "v_cvt_pk_bf16_f32 v202, v172, v173\n"
    "v_cvt_pk_bf16_f32 v203, v174, v175\n"
    "v_cvt_pk_bf16_f32 v204, v176, v177\n"
    "v_cvt_pk_bf16_f32 v205, v178, v179\n"
    "v_cvt_pk_bf16_f32 v206, v180, v181\n"
    "v_cvt_pk_bf16_f32 v207, v182, v183\n"
    "v_mfma_f32_32x32x16_bf16 v[224:239], v[128:131], v[200:203], v[224:239]\n"
    "s_nop 0\n"
    "v_mfma_f32_32x32x16_bf16 v[224:239], v[132:135], v[204:207], v[224:239]\n"
    "s_cmp_lt_u32 s75, 3\n"
    "s_cbranch_scc1 L_sl_a1_%=\n"
    "v_mfma_f32_32x32x16_bf16 v[168:183], v[80:83], v[64:67], v[208:223]\n"
    "s_nop 11\n"
    "v_exp_f32 v168, v168\n"
    "v_exp_f32 v169, v169\n"
    "v_exp_f32 v170, v170\n"
    "v_exp_f32 v171, v171\n"
    "v_exp_f32 v172, v172\n"
    "v_exp_f32 v173, v173\n"
    "v_exp_f32 v174, v174\n"
    "v_exp_f32 v175, v175\n"
    "v_exp_f32 v176, v176\n"
    "v_exp_f32 v177, v177\n"
    "v_exp_f32 v178, v178\n"
    "v_exp_f32 v179, v179\n"
    "v_exp_f32 v180, v180\n"
    "v_exp_f32 v181, v181\n"
    "v_exp_f32 v182, v182\n"
    "v_exp_f32 v183, v183\n"
    "v_cvt_pk_bf16_f32 v200, v168, v169\n"
    "v_cvt_pk_bf16_f32 v201, v170, v171\n"
    "v_cvt_pk_bf16_f32 v202, v172, v173\n"
    "v_cvt_pk_bf16_f32 v203, v174, v175\n"
    "v_cvt_pk_bf16_f32 v204, v176, v177\n"
    "v_cvt_pk_bf16_f32 v205, v178, v179\n"
    "v_cvt_pk_bf16_f32 v206, v180, v181\n"
    "v_cvt_pk_bf16_f32 v207, v182, v183\n"
    "v_mfma_f32_32x32x16_bf16 v[224:239], v[120:123], v[200:203], v[224:239]\n"
    "s_nop 0\n"
    "v_mfma_f32_32x32x16_bf16 v[224:239], v[124:127], v[204:207], v[224:239]\n"
    "s_cmp_lt_u32 s75, 4\n"
    "s_cbranch_scc1 L_sl_a1_%=\n"
    "v_mfma_f32_32x32x16_bf16 v[168:183], v[76:79], v[64:67], v[208:223]\n"
    "s_nop 11\n"
    "v_exp_f32 v168, v168\n"
    "v_exp_f32 v169, v169\n"
    "v_exp_f32 v170, v170\n"
    "v_exp_f32 v171, v171\n"
    "v_exp_f32 v172, v172\n"
    "v_exp_f32 v173, v173\n"
    "v_exp_f32 v174, v174\n"
    "v_exp_f32 v175, v175\n"
    "v_exp_f32 v176, v176\n"
    "v_exp_f32 v177, v177\n"
    "v_exp_f32 v178, v178\n"
    "v_exp_f32 v179, v179\n"
    "v_exp_f32 v180, v180\n"
    "v_exp_f32 v181, v181\n"
    "v_exp_f32 v182, v182\n"
    "v_exp_f32 v183, v183\n"
    "v_cvt_pk_bf16_f32 v200, v168, v169\n"
    "v_cvt_pk_bf16_f32 v201, v170, v171\n"
    "v_cvt_pk_bf16_f32 v202, v172, v173\n"
    "v_cvt_pk_bf16_f32 v203, v174, v175\n"
    "v_cvt_pk_bf16_f32 v204, v176, v177\n"
    "v_cvt_pk_bf16_f32 v205, v178, v179\n"
    "v_cvt_pk_bf16_f32 v206, v180, v181\n"
    "v_cvt_pk_bf16_f32 v207, v182, v183\n"
    "v_mfma_f32_32x32x16_bf16 v[224:239], v[112:115], v[200:203], v[224:239]\n"
    "s_nop 0\n"
    "v_mfma_f32_32x32x16_bf16 v[224:239], v[116:119], v[204:207], v[224:239]\n"
    "s_cmp_lt_u32 s75, 4\n"
    "s_cbranch_scc1 L_sl_a1_%=\n"
    "v_cmp_gt_i32 s[88:89], 0, v36\n"
    "v_cmp_gt_i32 s[90:91], 1, v36\n"
    "v_cmp_gt_i32 s[48:49], 2, v36\n"
    "v_cmp_gt_i32 s[50:51], 3, v36\n"
    "v_cndmask_b32 v208, v37, v39, s[88:89]\n"
    "v_cmp_gt_i32 s[88:89], 8, v36\n"
    "v_cndmask_b32 v209, v37, v39, s[90:91]\n"
    "v_cmp_gt_i32 s[90:91], 9, v36\n"
    "v_cndmask_b32 v210, v37, v39, s[48:49]\n"
    "v_cmp_gt_i32 s[48:49], 10, v36\n"
    "v_cndmask_b32 v211, v37, v39, s[50:51]\n"
    "v_cmp_gt_i32 s[50:51], 11, v36\n"
    "v_cndmask_b32 v212, v37, v39, s[88:89]\n"
    "v_cmp_gt_i32 s[88:89], 16, v36\n"
    "v_cndmask_b32 v213, v37, v39, s[90:91]\n"
    "v_cmp_gt_i32 s[90:91], 17, v36\n"
    "v_cndmask_b32 v214, v37, v39, s[48:49]\n"
    "v_cmp_gt_i32 s[48:49], 18, v36\n"
    "v_cndmask_b32 v215, v37, v39, s[50:51]\n"
    "v_cmp_gt_i32 s[50:51], 19, v36\n"
    "v_cndmask_b32 v216, v37, v39, s[88:89]\n"
    "v_cmp_gt_i32 s[88:89], 24, v36\n"
    "v_cndmask_b32 v217, v37, v39, s[90:91]\n"
    "v_cmp_gt_i32 s[90:91], 25, v36\n"
    "v_cndmask_b32 v218, v37, v39, s[48:49]\n"
    "v_cmp_gt_i32 s[48:49], 26, v36\n"
    "v_cndmask_b32 v219, v37, v39, s[50:51]\n"
    "v_cmp_gt_i32 s[50:51], 27, v36\n"
    "v_cndmask_b32 v220, v37, v39, s[88:89]\n"
    "v_cndmask_b32 v221, v37, v39, s[90:91]\n"
    "v_cndmask_b32 v222, v37, v39, s[48:49]\n"
    "v_cndmask_b32 v223, v37, v39, s[50:51]\n"
    "s_nop 1\n"
    "v_mfma_f32_32x32x16_bf16 v[168:183], v[72:75], v[64:67], v[208:223]\n"
    "s_nop 11\n"
    "v_exp_f32 v168, v168\n"
    "v_exp_f32 v169, v169\n"
    "v_exp_f32 v170, v170\n"
    "v_exp_f32 v171, v171\n"
    "v_exp_f32 v172, v172\n"
    "v_exp_f32 v173, v173\n"
    "v_exp_f32 v174, v174\n"
    "v_exp_f32 v175, v175\n"
    "v_exp_f32 v176, v176\n"
    "v_exp_f32 v177, v177\n"
    "v_exp_f32 v178, v178\n"
    "v_exp_f32 v179, v179\n"
    "v_exp_f32 v180, v180\n"
    "v_exp_f32 v181, v181\n"
    "v_exp_f32 v182, v182\n"
    "v_exp_f32 v183, v183\n"
    "v_cvt_pk_bf16_f32 v200, v168, v169\n"
    "v_cvt_pk_bf16_f32 v201, v170, v171\n"
    "v_cvt_pk_bf16_f32 v202, v172, v173\n"
    "v_cvt_pk_bf16_f32 v203, v174, v175\n"
    "v_cvt_pk_bf16_f32 v204, v176, v177\n"
    "v_cvt_pk_bf16_f32 v205, v178, v179\n"
    "v_cvt_pk_bf16_f32 v206, v180, v181\n"
    "v_cvt_pk_bf16_f32 v207, v182, v183\n"
    "v_mfma_f32_32x32x16_bf16 v[224:239], v[104:107], v[200:203], v[224:239]\n"
    "s_nop 0\n"
    "v_mfma_f32_32x32x16_bf16 v[224:239], v[108:111], v[204:207], v[224:239]\n"
    "L_sl_a1_%=:\n"
    "s_waitcnt vmcnt(0) lgkmcnt(0)\n"
    "s_nop 15\n"
    "v_rcp_f32 v234, v232\n"
    "v_accvgpr_read_b32 v168, a0\n"
    "v_accvgpr_read_b32 v169, a1\n"
    "v_accvgpr_read_b32 v170, a2\n"
    "v_accvgpr_read_b32 v171, a3\n"
    "v_accvgpr_read_b32 v172, a4\n"
    "v_accvgpr_read_b32 v173, a5\n"
    "v_accvgpr_read_b32 v174, a6\n"
    "v_accvgpr_read_b32 v175, a7\n"
    "v_accvgpr_read_b32 v176, a8\n"
    "v_accvgpr_read_b32 v177, a9\n"
    "v_accvgpr_read_b32 v178, a10\n"
    "v_accvgpr_read_b32 v179, a11\n"
    "v_accvgpr_read_b32 v180, a12\n"
    "v_accvgpr_read_b32 v181, a13\n"
    "v_accvgpr_read_b32 v182, a14\n"
    "v_accvgpr_read_b32 v183, a15\n"
    "v_accvgpr_read_b32 v184, a16\n"
    "v_accvgpr_read_b32 v185, a17\n"
    "v_accvgpr_read_b32 v186, a18\n"
    "v_accvgpr_read_b32 v187, a19\n"
    "v_accvgpr_read_b32 v188, a20\n"
    "v_accvgpr_read_b32 v189, a21\n"
    "v_accvgpr_read_b32 v190, a22\n"
    "v_accvgpr_read_b32 v191, a23\n"
    "v_accvgpr_read_b32 v192, a24\n"
    "v_accvgpr_read_b32 v193, a25\n"
    "v_accvgpr_read_b32 v194, a26\n"
    "v_accvgpr_read_b32 v195, a27\n"
    "v_accvgpr_read_b32 v196, a28\n"
    "v_accvgpr_read_b32 v197, a29\n"
    "v_accvgpr_read_b32 v198, a30\n"
    "v_accvgpr_read_b32 v199, a31\n"
    "v_accvgpr_read_b32 v200, a32\n"
    "v_accvgpr_read_b32 v201, a33\n"
    "v_accvgpr_read_b32 v202, a34\n"
    "v_accvgpr_read_b32 v203, a35\n"
    "v_accvgpr_read_b32 v204, a36\n"
    "v_accvgpr_read_b32 v205, a37\n"
    "v_accvgpr_read_b32 v206, a38\n"
    "v_accvgpr_read_b32 v207, a39\n"
    "v_accvgpr_read_b32 v208, a40\n"
    "v_accvgpr_read_b32 v209, a41\n"
    "v_accvgpr_read_b32 v210, a42\n"
    "v_accvgpr_read_b32 v211, a43\n"
    "v_accvgpr_read_b32 v212, a44\n"
    "v_accvgpr_read_b32 v213, a45\n"
    "v_accvgpr_read_b32 v214, a46\n"
    "v_accvgpr_read_b32 v215, a47\n"
    "v_accvgpr_read_b32 v216, a48\n"
    "v_accvgpr_read_b32 v217, a49\n"
    "v_accvgpr_read_b32 v218, a50\n"
    "v_accvgpr_read_b32 v219, a51\n"
    "v_accvgpr_read_b32 v220, a52\n"
    "v_accvgpr_read_b32 v221, a53\n"
    "v_accvgpr_read_b32 v222, a54\n"
    "v_accvgpr_read_b32 v223, a55\n"
    "s_nop 3\n"
    "s_branch L_att_donea_%=\n"
    "L_slowb_%=:\n"
    "s_waitcnt vmcnt(0) lgkmcnt(0)\n"
    "s_nop 15\n"
    "v_accvgpr_write_b32 a0, v168\n"
    "v_accvgpr_write_b32 a1, v169\n"
    "v_accvgpr_write_b32 a2, v170\n"
    "v_accvgpr_write_b32 a3, v171\n"
    "v_accvgpr_write_b32 a4, v172\n"
    "v_accvgpr_write_b32 a5, v173\n"
    "v_accvgpr_write_b32 a6, v174\n"
    "v_accvgpr_write_b32 a7, v175\n"
    "v_accvgpr_write_b32 a8, v176\n"
    "v_accvgpr_write_b32 a9, v177\n"
    "v_accvgpr_write_b32 a10, v178\n"
    "v_accvgpr_write_b32 a11, v179\n"
    "v_accvgpr_write_b32 a12, v180\n"
    "v_accvgpr_write_b32 a13, v181\n"
    "v_accvgpr_write_b32 a14, v182\n"
    "v_accvgpr_write_b32 a15, v183\n"
    "v_accvgpr_write_b32 a16, v184\n"
    "v_accvgpr_write_b32 a17, v185\n"
    "v_accvgpr_write_b32 a18, v186\n"
    "v_accvgpr_write_b32 a19, v187\n"
    "v_accvgpr_write_b32 a20, v188\n"
    "v_accvgpr_write_b32 a21, v189\n"
    "v_accvgpr_write_b32 a22, v190\n"
    "v_accvgpr_write_b32 a23, v191\n"
    "v_accvgpr_write_b32 a24, v192\n"
    "v_accvgpr_write_b32 a25, v193\n"
    "v_accvgpr_write_b32 a26, v194\n"
    "v_accvgpr_write_b32 a27, v195\n"
    "v_accvgpr_write_b32 a28, v196\n"
    "v_accvgpr_write_b32 a29, v197\n"
    "v_accvgpr_write_b32 a30, v198\n"
    "v_accvgpr_write_b32 a31, v199\n"
    "v_accvgpr_write_b32 a32, v200\n"
    "v_accvgpr_write_b32 a33, v201\n"
    "v_accvgpr_write_b32 a34, v202\n"
    "v_accvgpr_write_b32 a35, v203\n"
    "v_accvgpr_write_b32 a36, v204\n"
    "v_accvgpr_write_b32 a37, v205\n"
    "v_accvgpr_write_b32 a38, v206\n"
    "v_accvgpr_write_b32 a39, v207\n"
    "v_accvgpr_write_b32 a40, v208\n"
    "v_accvgpr_write_b32 a41, v209\n"
    "v_accvgpr_write_b32 a42, v210\n"
    "v_accvgpr_write_b32 a43, v211\n"
    "v_accvgpr_write_b32 a44, v212\n"
    "v_accvgpr_write_b32 a45, v213\n"
    "v_accvgpr_write_b32 a46, v214\n"
    "v_accvgpr_write_b32 a47, v215\n"
    "v_accvgpr_write_b32 a48, v216\n"
    "v_accvgpr_write_b32 a49, v217\n"
    "v_accvgpr_write_b32 a50, v218\n"
    "v_accvgpr_write_b32 a51, v219\n"
    "v_accvgpr_write_b32 a52, v220\n"
    "v_accvgpr_write_b32 a53, v221\n"
    "v_accvgpr_write_b32 a54, v222\n"
    "v_accvgpr_write_b32 a55, v223\n"
    "s_nop 0\n"
    "v_mov_b32 v38, 0xf149f2ca\n"
    "v_mov_b32 v39, 0\n"
    "v_cmp_le_i32 s[88:89], 0, v36\n"
    "v_cmp_le_i32 s[90:91], 1, v36\n"
    "v_cmp_le_i32 s[48:49], 2, v36\n"
    "v_cmp_le_i32 s[50:51], 3, v36\n"
    "v_cndmask_b32 v208, v37, v39, s[88:89]\n"
    "v_cmp_le_i32 s[88:89], 8, v36\n"
    "v_cndmask_b32 v209, v37, v39, s[90:91]\n"
    "v_cmp_le_i32 s[90:91], 9, v36\n"
    "v_cndmask_b32 v210, v37, v39, s[48:49]\n"
    "v_cmp_le_i32 s[48:49], 10, v36\n"
    "v_cndmask_b32 v211, v37, v39, s[50:51]\n"
    "v_cmp_le_i32 s[50:51], 11, v36\n"
    "v_cndmask_b32 v212, v37, v39, s[88:89]\n"
    "v_cmp_le_i32 s[88:89], 16, v36\n"
    "v_cndmask_b32 v213, v37, v39, s[90:91]\n"
    "v_cmp_le_i32 s[90:91], 17, v36\n"
    "v_cndmask_b32 v214, v37, v39, s[48:49]\n"
    "v_cmp_le_i32 s[48:49], 18, v36\n"
    "v_cndmask_b32 v215, v37, v39, s[50:51]\n"
    "v_cmp_le_i32 s[50:51], 19, v36\n"
    "v_cndmask_b32 v216, v37, v39, s[88:89]\n"
    "v_cmp_le_i32 s[88:89], 24, v36\n"
    "v_cndmask_b32 v217, v37, v39, s[90:91]\n"
    "v_cmp_le_i32 s[90:91], 25, v36\n"
    "v_cndmask_b32 v218, v37, v39, s[48:49]\n"
    "v_cmp_le_i32 s[48:49], 26, v36\n"
    "v_cndmask_b32 v219, v37, v39, s[50:51]\n"
    "v_cmp_le_i32 s[50:51], 27, v36\n"
    "v_cndmask_b32 v220, v37, v39, s[88:89]\n"
    "v_cndmask_b32 v221, v37, v39, s[90:91]\n"
    "v_cndmask_b32 v222, v37, v39, s[48:49]\n"
    "v_cndmask_b32 v223, v37, v39, s[50:51]\n"
    "s_nop 1\n"
    "v_mfma_f32_32x32x16_bf16 v[168:183], v[44:47], v[68:71], v[208:223]\n"
    "s_nop 11\n"
    "v_max3_f32 v38, v38, v168, v169\n"
    "v_max3_f32 v38, v38, v170, v171\n"
    "v_max3_f32 v38, v38, v172, v173\n"
    "v_max3_f32 v38, v38, v174, v175\n"
    "v_max3_f32 v38, v38, v176, v177\n"
    "v_max3_f32 v38, v38, v178, v179\n"
    "v_max3_f32 v38, v38, v180, v181\n"
    "v_max3_f32 v38, v38, v182, v183\n"
    "v_mov_b32 v208, v39\n"
    "v_mov_b32 v209, v39\n"
    "v_mov_b32 v210, v39\n"
    "v_mov_b32 v211, v39\n"
    "v_mov_b32 v212, v39\n"
    "v_mov_b32 v213, v39\n"
    "v_mov_b32 v214, v39\n"
    "v_mov_b32 v215, v39\n"
    "v_mov_b32 v216, v39\n"
    "v_mov_b32 v217, v39\n"
    "v_mov_b32 v218, v39\n"
    "v_mov_b32 v219, v39\n"
    "v_mov_b32 v220, v39\n"
    "v_mov_b32 v221, v39\n"
    "v_mov_b32 v222, v39\n"
    "v_mov_b32 v223, v39\n"
    "s_nop 1\n"
    "v_mfma_f32_32x32x16_bf16 v[168:183], v[40:43], v[68:71], v[208:223]\n"
    "s_nop 11\n"
    "v_max3_f32 v38, v38, v168, v169\n"
    "v_max3_f32 v38, v38, v170, v171\n"
    "v_max3_f32 v38, v38, v172, v173\n"
    "v_max3_f32 v38, v38, v174, v175\n"
    "v_max3_f32 v38, v38, v176, v177\n"
    "v_max3_f32 v38, v38, v178, v179\n"
    "v_max3_f32 v38, v38, v180, v181\n"
    "v_max3_f32 v38, v38, v182, v183\n"
    "s_cmp_lt_u32 s75, 1\n"
    "s_cbranch_scc1 L_sl_b0_%=\n"
    "v_mfma_f32_32x32x16_bf16 v[168:183], v[100:103], v[68:71], v[208:223]\n"
    "s_nop 11\n"
    "v_max3_f32 v38, v38, v168, v169\n"
    "v_max3_f32 v38, v38, v170, v171\n"
    "v_max3_f32 v38, v38, v172, v173\n"
    "v_max3_f32 v38, v38, v174, v175\n"
    "v_max3_f32 v38, v38, v176, v177\n"
    "v_max3_f32 v38, v38, v178, v179\n"
    "v_max3_f32 v38, v38, v180, v181\n"
    "v_max3_f32 v38, v38, v182, v183\n"
    "s_cmp_lt_u32 s75, 1\n"
    "s_cbranch_scc1 L_sl_b0_%=\n"
    "v_mfma_f32_32x32x16_bf16 v[168:183], v[96:99], v[68:71], v[208:223]\n"
    "s_nop 11\n"
    "v_max3_f32 v38, v38, v168, v169\n"
    "v_max3_f32 v38, v38, v170, v171\n"
    "v_max3_f32 v38, v38, v172, v173\n"
    "v_max3_f32 v38, v38, v174, v175\n"
    "v_max3_f32 v38, v38, v176, v177\n"
    "v_max3_f32 v38, v38, v178, v179\n"
    "v_max3_f32 v38, v38, v180, v181\n"
    "v_max3_f32 v38, v38, v182, v183\n"
    "s_cmp_lt_u32 s75, 2\n"
    "s_cbranch_scc1 L_sl_b0_%=\n"
    "v_mfma_f32_32x32x16_bf16 v[168:183], v[92:95], v[68:71], v[208:223]\n"
    "s_nop 11\n"
    "v_max3_f32 v38, v38, v168, v169\n"
    "v_max3_f32 v38, v38, v170, v171\n"
    "v_max3_f32 v38, v38, v172, v173\n"
    "v_max3_f32 v38, v38, v174, v175\n"
    "v_max3_f32 v38, v38, v176, v177\n"
    "v_max3_f32 v38, v38, v178, v179\n"
    "v_max3_f32 v38, v38, v180, v181\n"
    "v_max3_f32 v38, v38, v182, v183\n"
    "s_cmp_lt_u32 s75, 2\n"
    "s_cbranch_scc1 L_sl_b0_%=\n"
    "v_mfma_f32_32x32x16_bf16 v[168:183], v[88:91], v[68:71], v[208:223]\n"
    "s_nop 11\n"
    "v_max3_f32 v38, v38, v168, v169\n"
    "v_max3_f32 v38, v38, v170, v171\n"
    "v_max3_f32 v38, v38, v172, v173\n"
    "v_max3_f32 v38, v38, v174, v175\n"
    "v_max3_f32 v38, v38, v176, v177\n"
    "v_max3_f32 v38, v38, v178, v179\n"
    "v_max3_f32 v38, v38, v180, v181\n"
    "v_max3_f32 v38, v38, v182, v183\n"
    "s_cmp_lt_u32 s75, 3\n"
    "s_cbranch_scc1 L_sl_b0_%=\n"
    "v_mfma_f32_32x32x16_bf16 v[168:183], v[84:87], v[68:71], v[208:223]\n"
    "s_nop 11\n"
    "v_max3_f32 v38, v38, v168, v169\n"
    "v_max3_f32 v38, v38, v170, v171\n"
    "v_max3_f32 v38, v38, v172, v173\n"
    "v_max3_f32 v38, v38, v174, v175\n"
    "v_max3_f32 v38, v38, v176, v177\n"
    "v_max3_f32 v38, v38, v178, v179\n"
    "v_max3_f32 v38, v38, v180, v181\n"
    "v_max3_f32 v38, v38, v182, v183\n"
    "s_cmp_lt_u32 s75, 3\n"
    "s_cbranch_scc1 L_sl_b0_%=\n"
    "v_mfma_f32_32x32x16_bf16 v[168:183], v[80:83], v[68:71], v[208:223]\n"
    "s_nop 11\n"
    "v_max3_f32 v38, v38, v168, v169\n"
    "v_max3_f32 v38, v38, v170, v171\n"
    "v_max3_f32 v38, v38, v172, v173\n"
    "v_max3_f32 v38, v38, v174, v175\n"
    "v_max3_f32 v38, v38, v176, v177\n"
    "v_max3_f32 v38, v38, v178, v179\n"
    "v_max3_f32 v38, v38, v180, v181\n"
    "v_max3_f32 v38, v38, v182, v183\n"
    "s_cmp_lt_u32 s75, 4\n"
    "s_cbranch_scc1 L_sl_b0_%=\n"
    "v_cmp_gt_i32 s[88:89], 0, v36\n"
    "v_cmp_gt_i32 s[90:91], 1, v36\n"
    "v_cmp_gt_i32 s[48:49], 2, v36\n"
    "v_cmp_gt_i32 s[50:51], 3, v36\n"
    "v_cndmask_b32 v208, v37, v39, s[88:89]\n"
    "v_cmp_gt_i32 s[88:89], 8, v36\n"
    "v_cndmask_b32 v209, v37, v39, s[90:91]\n"
    "v_cmp_gt_i32 s[90:91], 9, v36\n"
    "v_cndmask_b32 v210, v37, v39, s[48:49]\n"
    "v_cmp_gt_i32 s[48:49], 10, v36\n"
    "v_cndmask_b32 v211, v37, v39, s[50:51]\n"
    "v_cmp_gt_i32 s[50:51], 11, v36\n"
    "v_cndmask_b32 v212, v37, v39, s[88:89]\n"
    "v_cmp_gt_i32 s[88:89], 16, v36\n"
    "v_cndmask_b32 v213, v37, v39, s[90:91]\n"
    "v_cmp_gt_i32 s[90:91], 17, v36\n"
    "v_cndmask_b32 v214, v37, v39, s[48:49]\n"
    "v_cmp_gt_i32 s[48:49], 18, v36\n"
    "v_cndmask_b32 v215, v37, v39, s[50:51]\n"
    "v_cmp_gt_i32 s[50:51], 19, v36\n"
    "v_cndmask_b32 v216, v37, v39, s[88:89]\n"
    "v_cmp_gt_i32 s[88:89], 24, v36\n"
    "v_cndmask_b32 v217, v37, v39, s[90:91]\n"
    "v_cmp_gt_i32 s[90:91], 25, v36\n"
    "v_cndmask_b32 v218, v37, v39, s[48:49]\n"
    "v_cmp_gt_i32 s[48:49], 26, v36\n"
    "v_cndmask_b32 v219, v37, v39, s[50:51]\n"
    "v_cmp_gt_i32 s[50:51], 27, v36\n"
    "v_cndmask_b32 v220, v37, v39, s[88:89]\n"
    "v_cndmask_b32 v221, v37, v39, s[90:91]\n"
    "v_cndmask_b32 v222, v37, v39, s[48:49]\n"
    "v_cndmask_b32 v223, v37, v39, s[50:51]\n"
    "s_nop 1\n"
    "v_mfma_f32_32x32x16_bf16 v[168:183], v[76:79], v[68:71], v[208:223]\n"
    "s_nop 11\n"
    "v_max3_f32 v38, v38, v168, v169\n"
    "v_max3_f32 v38, v38, v170, v171\n"
    "v_max3_f32 v38, v38, v172, v173\n"
    "v_max3_f32 v38, v38, v174, v175\n"
    "v_max3_f32 v38, v38, v176, v177\n"
    "v_max3_f32 v38, v38, v178, v179\n"
    "v_max3_f32 v38, v38, v180, v181\n"
    "v_max3_f32 v38, v38, v182, v183\n"
    "L_sl_b0_%=:\n"
    "s_waitcnt vmcnt(0) lgkmcnt(0)\n"
    "s_nop 15\n"
    "v_mov_b32 v39, v38\n"
    "s_nop 1\n"
    "v_permlane32_swap_b32 v38, v39\n"
    "v_max_f32 v39, v38, v39\n"
    "v_xor_b32 v39, 0x80000000, v39\n"
    "v_cmp_le_i32 s[88:89], 0, v36\n"
    "v_cmp_le_i32 s[90:91], 1, v36\n"
    "v_cmp_le_i32 s[48:49], 2, v36\n"
    "v_cmp_le_i32 s[50:51], 3, v36\n"
    "v_cndmask_b32 v208, v37, v39, s[88:89]\n"
    "v_cmp_le_i32 s[88:89], 8, v36\n"
    "v_cndmask_b32 v209, v37, v39, s[90:91]\n"
    "v_cmp_le_i32 s[90:91], 9, v36\n"
    "v_cndmask_b32 v210, v37, v39, s[48:49]\n"
    "v_cmp_le_i32 s[48:49], 10, v36\n"
    "v_cndmask_b32 v211, v37, v39, s[50:51]\n"
    "v_cmp_le_i32 s[50:51], 11, v36\n"
    "v_cndmask_b32 v212, v37, v39, s[88:89]\n"
    "v_cmp_le_i32 s[88:89], 16, v36\n"
    "v_cndmask_b32 v213, v37, v39, s[90:91]\n"
    "v_cmp_le_i32 s[90:91], 17, v36\n"
    "v_cndmask_b32 v214, v37, v39, s[48:49]\n"
    "v_cmp_le_i32 s[48:49], 18, v36\n"
    "v_cndmask_b32 v215, v37, v39, s[50:51]\n"
    "v_cmp_le_i32 s[50:51], 19, v36\n"
    "v_cndmask_b32 v216, v37, v39, s[88:89]\n"
    "v_cmp_le_i32 s[88:89], 24, v36\n"
    "v_cndmask_b32 v217, v37, v39, s[90:91]\n"
    "v_cmp_le_i32 s[90:91], 25, v36\n"
    "v_cndmask_b32 v218, v37, v39, s[48:49]\n"
    "v_cmp_le_i32 s[48:49], 26, v36\n"
    "v_cndmask_b32 v219, v37, v39, s[50:51]\n"
    "v_cmp_le_i32 s[50:51], 27, v36\n"
    "v_cndmask_b32 v220, v37, v39, s[88:89]\n"
    "v_cndmask_b32 v221, v37, v39, s[90:91]\n"
    "v_cndmask_b32 v222, v37, v39, s[48:49]\n"
    "v_cndmask_b32 v223, v37, v39, s[50:51]\n"
    "s_nop 1\n"
    "v_mfma_f32_32x32x16_bf16 v[168:183], v[44:47], v[68:71], v[208:223]\n"
    "s_nop 11\n"
    "v_exp_f32 v168, v168\n"
    "v_exp_f32 v169, v169\n"
    "v_exp_f32 v170, v170\n"
    "v_exp_f32 v171, v171\n"
    "v_exp_f32 v172, v172\n"
    "v_exp_f32 v173, v173\n"
    "v_exp_f32 v174, v174\n"
    "v_exp_f32 v175, v175\n"
    "v_exp_f32 v176, v176\n"
    "v_exp_f32 v177, v177\n"
    "v_exp_f32 v178, v178\n"
    "v_exp_f32 v179, v179\n"
    "v_exp_f32 v180, v180\n"
    "v_exp_f32 v181, v181\n"
    "v_exp_f32 v182, v182\n"
    "v_exp_f32 v183, v183\n"
    "v_cvt_pk_bf16_f32 v200, v168, v169\n"
    "v_cvt_pk_bf16_f32 v201, v170, v171\n"
    "v_cvt_pk_bf16_f32 v202, v172, v173\n"
    "v_cvt_pk_bf16_f32 v203, v174, v175\n"
    "v_cvt_pk_bf16_f32 v204, v176, v177\n"
    "v_cvt_pk_bf16_f32 v205, v178, v179\n"
    "v_cvt_pk_bf16_f32 v206, v180, v181\n"
    "v_cvt_pk_bf16_f32 v207, v182, v183\n"
    "v_mfma_f32_32x32x16_bf16 v[240:255], v[56:59], v[200:203], 0\n"
    "s_nop 0\n"
    "v_mfma_f32_32x32x16_bf16 v[240:255], v[60:63], v[204:207], v[240:255]\n"
    "v_mov_b32 v208, v39\n"
    "v_mov_b32 v209, v39\n"
    "v_mov_b32 v210, v39\n"
    "v_mov_b32 v211, v39\n"
    "v_mov_b32 v212, v39\n"
    "v_mov_b32 v213, v39\n"
    "v_mov_b32 v214, v39\n"
    "v_mov_b32 v215, v39\n"
    "v_mov_b32 v216, v39\n"
    "v_mov_b32 v217, v39\n"
    "v_mov_b32 v218, v39\n"
    "v_mov_b32 v219, v39\n"
    "v_mov_b32 v220, v39\n"
    "v_mov_b32 v221, v39\n"
    "v_mov_b32 v222, v39\n"
    "v_mov_b32 v223, v39\n"
    "s_nop 1\n"
    "v_mfma_f32_32x32x16_bf16 v[168:183], v[40:43], v[68:71], v[208:223]\n"
    "s_nop 11\n"
    "v_exp_f32 v168, v168\n"
    "v_exp_f32 v169, v169\n"
    "v_exp_f32 v170, v170\n"
    "v_exp_f32 v171, v171\n"
    "v_exp_f32 v172, v172\n"
    "v_exp_f32 v173, v173\n"
    "v_exp_f32 v174, v174\n"
    "v_exp_f32 v175, v175\n"
    "v_exp_f32 v176, v176\n"
    "v_exp_f32 v177, v177\n"
    "v_exp_f32 v178, v178\n"
    "v_exp_f32 v179, v179\n"
    "v_exp_f32 v180, v180\n"
    "v_exp_f32 v181, v181\n"
    "v_exp_f32 v182, v182\n"
    "v_exp_f32 v183, v183\n"
    "v_cvt_pk_bf16_f32 v200, v168, v169\n"
    "v_cvt_pk_bf16_f32 v201, v170, v171\n"
    "v_cvt_pk_bf16_f32 v202, v172, v173\n"
    "v_cvt_pk_bf16_f32 v203, v174, v175\n"
    "v_cvt_pk_bf16_f32 v204, v176, v177\n"
    "v_cvt_pk_bf16_f32 v205, v178, v179\n"
    "v_cvt_pk_bf16_f32 v206, v180, v181\n"
    "v_cvt_pk_bf16_f32 v207, v182, v183\n"
    "v_mfma_f32_32x32x16_bf16 v[240:255], v[48:51], v[200:203], v[240:255]\n"
    "s_nop 0\n"
    "v_mfma_f32_32x32x16_bf16 v[240:255], v[52:55], v[204:207], v[240:255]\n"
    "s_cmp_lt_u32 s75, 1\n"
    "s_cbranch_scc1 L_sl_b1_%=\n"
    "v_mfma_f32_32x32x16_bf16 v[168:183], v[100:103], v[68:71], v[208:223]\n"
    "s_nop 11\n"
    "v_exp_f32 v168, v168\n"
    "v_exp_f32 v169, v169\n"
    "v_exp_f32 v170, v170\n"
    "v_exp_f32 v171, v171\n"
    "v_exp_f32 v172, v172\n"
    "v_exp_f32 v173, v173\n"
    "v_exp_f32 v174, v174\n"
    "v_exp_f32 v175, v175\n"
    "v_exp_f32 v176, v176\n"
    "v_exp_f32 v177, v177\n"
    "v_exp_f32 v178, v178\n"
    "v_exp_f32 v179, v179\n"
    "v_exp_f32 v180, v180\n"
    "v_exp_f32 v181, v181\n"
    "v_exp_f32 v182, v182\n"
    "v_exp_f32 v183, v183\n"
    "v_cvt_pk_bf16_f32 v200, v168, v169\n"
    "v_cvt_pk_bf16_f32 v201, v170, v171\n"
    "v_cvt_pk_bf16_f32 v202, v172, v173\n"
    "v_cvt_pk_bf16_f32 v203, v174, v175\n"
    "v_cvt_pk_bf16_f32 v204, v176, v177\n"
    "v_cvt_pk_bf16_f32 v205, v178, v179\n"
    "v_cvt_pk_bf16_f32 v206, v180, v181\n"
    "v_cvt_pk_bf16_f32 v207, v182, v183\n"
    "v_mfma_f32_32x32x16_bf16 v[240:255], v[160:163], v[200:203], v[240:255]\n"
    "s_nop 0\n"
    "v_mfma_f32_32x32x16_bf16 v[240:255], v[164:167], v[204:207], v[240:255]\n"
    "s_cmp_lt_u32 s75, 1\n"
    "s_cbranch_scc1 L_sl_b1_%=\n"
    "v_mfma_f32_32x32x16_bf16 v[168:183], v[96:99], v[68:71], v[208:223]\n"
    "s_nop 11\n"
    "v_exp_f32 v168, v168\n"
    "v_exp_f32 v169, v169\n"
    "v_exp_f32 v170, v170\n"
    "v_exp_f32 v171, v171\n"
    "v_exp_f32 v172, v172\n"
    "v_exp_f32 v173, v173\n"
    "v_exp_f32 v174, v174\n"
    "v_exp_f32 v175, v175\n"
    "v_exp_f32 v176, v176\n"
    "v_exp_f32 v177, v177\n"
    "v_exp_f32 v178, v178\n"
    "v_exp_f32 v179, v179\n"
    "v_exp_f32 v180, v180\n"
    "v_exp_f32 v181, v181\n"
    "v_exp_f32 v182, v182\n"
    "v_exp_f32 v183, v183\n"
    "v_cvt_pk_bf16_f32 v200, v168, v169\n"
    "v_cvt_pk_bf16_f32 v201, v170, v171\n"
    "v_cvt_pk_bf16_f32 v202, v172, v173\n"
    "v_cvt_pk_bf16_f32 v203, v174, v175\n"
    "v_cvt_pk_bf16_f32 v204, v176, v177\n"
    "v_cvt_pk_bf16_f32 v205, v178, v179\n"
    "v_cvt_pk_bf16_f32 v206, v180, v181\n"
    "v_cvt_pk_bf16_f32 v207, v182, v183\n"
    "v_mfma_f32_32x32x16_bf16 v[240:255], v[152:155], v[200:203], v[240:255]\n"
    "s_nop 0\n"
    "v_mfma_f32_32x32x16_bf16 v[240:255], v[156:159], v[204:207], v[240:255]\n"
    "s_cmp_lt_u32 s75, 2\n"
    "s_cbranch_scc1 L_sl_b1_%=\n"
    "v_mfma_f32_32x32x16_bf16 v[168:183], v[92:95], v[68:71], v[208:223]\n"
    "s_nop 11\n"
    "v_exp_f32 v168, v168\n"
    "v_exp_f32 v169, v169\n"
    "v_exp_f32 v170, v170\n"
    "v_exp_f32 v171, v171\n"
    "v_exp_f32 v172, v172\n"
    "v_exp_f32 v173, v173\n"
    "v_exp_f32 v174, v174\n"
    "v_exp_f32 v175, v175\n"
    "v_exp_f32 v176, v176\n"
    "v_exp_f32 v177, v177\n"
    "v_exp_f32 v178, v178\n"
    "v_exp_f32 v179, v179\n"
    "v_exp_f32 v180, v180\n"
    "v_exp_f32 v181, v181\n"
    "v_exp_f32 v182, v182\n"
    "v_exp_f32 v183, v183\n"
    "v_cvt_pk_bf16_f32 v200, v168, v169\n"
    "v_cvt_pk_bf16_f32 v201, v170, v171\n"
    "v_cvt_pk_bf16_f32 v202, v172, v173\n"
    "v_cvt_pk_bf16_f32 v203, v174, v175\n"
    "v_cvt_pk_bf16_f32 v204, v176, v177\n"
    "v_cvt_pk_bf16_f32 v205, v178, v179\n"
    "v_cvt_pk_bf16_f32 v206, v180, v181\n"
    "v_cvt_pk_bf16_f32 v207, v182, v183\n"
    "v_mfma_f32_32x32x16_bf16 v[240:255], v[144:147], v[200:203], v[240:255]\n"
    "s_nop 0\n"
    "v_mfma_f32_32x32x16_bf16 v[240:255], v[148:151], v[204:207], v[240:255]\n"
    "s_cmp_lt_u32 s75, 2\n"
    "s_cbranch_scc1 L_sl_b1_%=\n"
    "v_mfma_f32_32x32x16_bf16 v[168:183], v[88:91], v[68:71], v[208:223]\n"
    "s_nop 11\n"
    "v_exp_f32 v168, v168\n"
    "v_exp_f32 v169, v169\n"
    "v_exp_f32 v170, v170\n"
    "v_exp_f32 v171, v171\n"
    "v_exp_f32 v172, v172\n"
    "v_exp_f32 v173, v173\n"
    "v_exp_f32 v174, v174\n"
    "v_exp_f32 v175, v175\n"
    "v_exp_f32 v176, v176\n"
    "v_exp_f32 v177, v177\n"
    "v_exp_f32 v178, v178\n"
    "v_exp_f32 v179, v179\n"
    "v_exp_f32 v180, v180\n"
    "v_exp_f32 v181, v181\n"
    "v_exp_f32 v182, v182\n"
    "v_exp_f32 v183, v183\n"
    "v_cvt_pk_bf16_f32 v200, v168, v169\n"
    "v_cvt_pk_bf16_f32 v201, v170, v171\n"
    "v_cvt_pk_bf16_f32 v202, v172, v173\n"
    "v_cvt_pk_bf16_f32 v203, v174, v175\n"
    "v_cvt_pk_bf16_f32 v204, v176, v177\n"
    "v_cvt_pk_bf16_f32 v205, v178, v179\n"
    "v_cvt_pk_bf16_f32 v206, v180, v181\n"
    "v_cvt_pk_bf16_f32 v207, v182, v183\n"
    "v_mfma_f32_32x32x16_bf16 v[240:255], v[136:139], v[200:203], v[240:255]\n"
    "s_nop 0\n"
    "v_mfma_f32_32x32x16_bf16 v[240:255], v[140:143], v[204:207], v[240:255]\n"
    "s_cmp_lt_u32 s75, 3\n"
    "s_cbranch_scc1 L_sl_b1_%=\n"
    "v_mfma_f32_32x32x16_bf16 v[168:183], v[84:87], v[68:71], v[208:223]\n"
    "s_nop 11\n"
    "v_exp_f32 v168, v168\n"
    "v_exp_f32 v169, v169\n"
    "v_exp_f32 v170, v170\n"
    "v_exp_f32 v171, v171\n"
    "v_exp_f32 v172, v172\n"
    "v_exp_f32 v173, v173\n"
    "v_exp_f32 v174, v174\n"
    "v_exp_f32 v175, v175\n"
    "v_exp_f32 v176, v176\n"
    "v_exp_f32 v177, v177\n"
    "v_exp_f32 v178, v178\n"
    "v_exp_f32 v179, v179\n"
    "v_exp_f32 v180, v180\n"
    "v_exp_f32 v181, v181\n"
    "v_exp_f32 v182, v182\n"
    "v_exp_f32 v183, v183\n"
    "v_cvt_pk_bf16_f32 v200, v168, v169\n"
    "v_cvt_pk_bf16_f32 v201, v170, v171\n"
    "v_cvt_pk_bf16_f32 v202, v172, v173\n"
    "v_cvt_pk_bf16_f32 v203, v174, v175\n"
    "v_cvt_pk_bf16_f32 v204, v176, v177\n"
    "v_cvt_pk_bf16_f32 v205, v178, v179\n"
    "v_cvt_pk_bf16_f32 v206, v180, v181\n"
    "v_cvt_pk_bf16_f32 v207, v182, v183\n"
    "v_mfma_f32_32x32x16_bf16 v[240:255], v[128:131], v[200:203], v[240:255]\n"
    "s_nop 0\n"
    "v_mfma_f32_32x32x16_bf16 v[240:255], v[132:135], v[204:207], v[240:255]\n"
    "s_cmp_lt_u32 s75, 3\n"
    "s_cbranch_scc1 L_sl_b1_%=\n"
    "v_mfma_f32_32x32x16_bf16 v[168:183], v[80:83], v[68:71], v[208:223]\n"
    "s_nop 11\n"
    "v_exp_f32 v168, v168\n"
    "v_exp_f32 v169, v169\n"
    "v_exp_f32 v170, v170\n"
    "v_exp_f32 v171, v171\n"
    "v_exp_f32 v172, v172\n"
    "v_exp_f32 v173, v173\n"
    "v_exp_f32 v174, v174\n"
    "v_exp_f32 v175, v175\n"
    "v_exp_f32 v176, v176\n"
    "v_exp_f32 v177, v177\n"
    "v_exp_f32 v178, v178\n"
    "v_exp_f32 v179, v179\n"
    "v_exp_f32 v180, v180\n"
    "v_exp_f32 v181, v181\n"
    "v_exp_f32 v182, v182\n"
    "v_exp_f32 v183, v183\n"
    "v_cvt_pk_bf16_f32 v200, v168, v169\n"
    "v_cvt_pk_bf16_f32 v201, v170, v171\n"
    "v_cvt_pk_bf16_f32 v202, v172, v173\n"
    "v_cvt_pk_bf16_f32 v203, v174, v175\n"
    "v_cvt_pk_bf16_f32 v204, v176, v177\n"
    "v_cvt_pk_bf16_f32 v205, v178, v179\n"
    "v_cvt_pk_bf16_f32 v206, v180, v181\n"
    "v_cvt_pk_bf16_f32 v207, v182, v183\n"
    "v_mfma_f32_32x32x16_bf16 v[240:255], v[120:123], v[200:203], v[240:255]\n"
    "s_nop 0\n"
    "v_mfma_f32_32x32x16_bf16 v[240:255], v[124:127], v[204:207], v[240:255]\n"
    "s_cmp_lt_u32 s75, 4\n"
    "s_cbranch_scc1 L_sl_b1_%=\n"
    "v_cmp_gt_i32 s[88:89], 0, v36\n"
    "v_cmp_gt_i32 s[90:91], 1, v36\n"
    "v_cmp_gt_i32 s[48:49], 2, v36\n"
    "v_cmp_gt_i32 s[50:51], 3, v36\n"
    "v_cndmask_b32 v208, v37, v39, s[88:89]\n"
    "v_cmp_gt_i32 s[88:89], 8, v36\n"
    "v_cndmask_b32 v209, v37, v39, s[90:91]\n"
    "v_cmp_gt_i32 s[90:91], 9, v36\n"
    "v_cndmask_b32 v210, v37, v39, s[48:49]\n"
    "v_cmp_gt_i32 s[48:49], 10, v36\n"
    "v_cndmask_b32 v211, v37, v39, s[50:51]\n"
    "v_cmp_gt_i32 s[50:51], 11, v36\n"
    "v_cndmask_b32 v212, v37, v39, s[88:89]\n"
    "v_cmp_gt_i32 s[88:89], 16, v36\n"
    "v_cndmask_b32 v213, v37, v39, s[90:91]\n"
    "v_cmp_gt_i32 s[90:91], 17, v36\n"
    "v_cndmask_b32 v214, v37, v39, s[48:49]\n"
    "v_cmp_gt_i32 s[48:49], 18, v36\n"
    "v_cndmask_b32 v215, v37, v39, s[50:51]\n"
    "v_cmp_gt_i32 s[50:51], 19, v36\n"
    "v_cndmask_b32 v216, v37, v39, s[88:89]\n"
    "v_cmp_gt_i32 s[88:89], 24, v36\n"
    "v_cndmask_b32 v217, v37, v39, s[90:91]\n"
    "v_cmp_gt_i32 s[90:91], 25, v36\n"
    "v_cndmask_b32 v218, v37, v39, s[48:49]\n"
    "v_cmp_gt_i32 s[48:49], 26, v36\n"
    "v_cndmask_b32 v219, v37, v39, s[50:51]\n"
    "v_cmp_gt_i32 s[50:51], 27, v36\n"
    "v_cndmask_b32 v220, v37, v39, s[88:89]\n"
    "v_cndmask_b32 v221, v37, v39, s[90:91]\n"
    "v_cndmask_b32 v222, v37, v39, s[48:49]\n"
    "v_cndmask_b32 v223, v37, v39, s[50:51]\n"
    "s_nop 1\n"
    "v_mfma_f32_32x32x16_bf16 v[168:183], v[76:79], v[68:71], v[208:223]\n"
    "s_nop 11\n"
    "v_exp_f32 v168, v168\n"
    "v_exp_f32 v169, v169\n"
    "v_exp_f32 v170, v170\n"
    "v_exp_f32 v171, v171\n"
    "v_exp_f32 v172, v172\n"
    "v_exp_f32 v173, v173\n"
    "v_exp_f32 v174, v174\n"
    "v_exp_f32 v175, v175\n"
    "v_exp_f32 v176, v176\n"
    "v_exp_f32 v177, v177\n"
    "v_exp_f32 v178, v178\n"
    "v_exp_f32 v179, v179\n"
    "v_exp_f32 v180, v180\n"
    "v_exp_f32 v181, v181\n"
    "v_exp_f32 v182, v182\n"
    "v_exp_f32 v183, v183\n"
    "v_cvt_pk_bf16_f32 v200, v168, v169\n"
    "v_cvt_pk_bf16_f32 v201, v170, v171\n"
    "v_cvt_pk_bf16_f32 v202, v172, v173\n"
    "v_cvt_pk_bf16_f32 v203, v174, v175\n"
    "v_cvt_pk_bf16_f32 v204, v176, v177\n"
    "v_cvt_pk_bf16_f32 v205, v178, v179\n"
    "v_cvt_pk_bf16_f32 v206, v180, v181\n"
    "v_cvt_pk_bf16_f32 v207, v182, v183\n"
    "v_mfma_f32_32x32x16_bf16 v[240:255], v[112:115], v[200:203], v[240:255]\n"
    "s_nop 0\n"
    "v_mfma_f32_32x32x16_bf16 v[240:255], v[116:119], v[204:207], v[240:255]\n"
    "L_sl_b1_%=:\n"
    "s_waitcnt vmcnt(0) lgkmcnt(0)\n"
    "s_nop 15\n"
    "v_rcp_f32 v250, v248\n"
    "v_accvgpr_read_b32 v168, a0\n"
    "v_accvgpr_read_b32 v169, a1\n"
    "v_accvgpr_read_b32 v170, a2\n"
    "v_accvgpr_read_b32 v171, a3\n"
    "v_accvgpr_read_b32 v172, a4\n"
    "v_accvgpr_read_b32 v173, a5\n"
    "v_accvgpr_read_b32 v174, a6\n"
    "v_accvgpr_read_b32 v175, a7\n"
    "v_accvgpr_read_b32 v176, a8\n"
    "v_accvgpr_read_b32 v177, a9\n"
    "v_accvgpr_read_b32 v178, a10\n"
    "v_accvgpr_read_b32 v179, a11\n"
    "v_accvgpr_read_b32 v180, a12\n"
    "v_accvgpr_read_b32 v181, a13\n"
    "v_accvgpr_read_b32 v182, a14\n"
    "v_accvgpr_read_b32 v183, a15\n"
    "v_accvgpr_read_b32 v184, a16\n"
    "v_accvgpr_read_b32 v185, a17\n"
    "v_accvgpr_read_b32 v186, a18\n"
    "v_accvgpr_read_b32 v187, a19\n"
    "v_accvgpr_read_b32 v188, a20\n"
    "v_accvgpr_read_b32 v189, a21\n"
    "v_accvgpr_read_b32 v190, a22\n"
    "v_accvgpr_read_b32 v191, a23\n"
    "v_accvgpr_read_b32 v192, a24\n"
    "v_accvgpr_read_b32 v193, a25\n"
    "v_accvgpr_read_b32 v194, a26\n"
    "v_accvgpr_read_b32 v195, a27\n"
    "v_accvgpr_read_b32 v196, a28\n"
    "v_accvgpr_read_b32 v197, a29\n"
    "v_accvgpr_read_b32 v198, a30\n"
    "v_accvgpr_read_b32 v199, a31\n"
    "v_accvgpr_read_b32 v200, a32\n"
    "v_accvgpr_read_b32 v201, a33\n"
    "v_accvgpr_read_b32 v202, a34\n"
    "v_accvgpr_read_b32 v203, a35\n"
    "v_accvgpr_read_b32 v204, a36\n"
    "v_accvgpr_read_b32 v205, a37\n"
    "v_accvgpr_read_b32 v206, a38\n"
    "v_accvgpr_read_b32 v207, a39\n"
    "v_accvgpr_read_b32 v208, a40\n"
    "v_accvgpr_read_b32 v209, a41\n"
    "v_accvgpr_read_b32 v210, a42\n"
    "v_accvgpr_read_b32 v211, a43\n"
    "v_accvgpr_read_b32 v212, a44\n"
    "v_accvgpr_read_b32 v213, a45\n"
    "v_accvgpr_read_b32 v214, a46\n"
    "v_accvgpr_read_b32 v215, a47\n"
    "v_accvgpr_read_b32 v216, a48\n"
    "v_accvgpr_read_b32 v217, a49\n"
    "v_accvgpr_read_b32 v218, a50\n"
    "v_accvgpr_read_b32 v219, a51\n"
    "v_accvgpr_read_b32 v220, a52\n"
    "v_accvgpr_read_b32 v221, a53\n"
    "v_accvgpr_read_b32 v222, a54\n"
    "v_accvgpr_read_b32 v223, a55\n"
    "s_nop 3\n"
    "s_branch L_att_doneb_%=\n"
    "L_exit_%=:\n"
    :
    : "s"(__builtin_amdgcn_kernarg_segment_ptr()), "s"(blockIdx.x), "v"(threadIdx.x)
    : "v1", "v2", "v3", "v4", "v5", "v6", "v7", "v8", "v9", "v10", "v11", "v12", "v13", "v14", "v15", "v16", "v17", "v18", "v19", "v20", "v21", "v22", "v23", "v24", "v25", "v26", "v27", "v28", "v29", "v30", "v31", "v32", "v33", "v34", "v35", "v36", "v37", "v38", "v39", "v40", "v41", "v42", "v43", "v44", "v45", "v46", "v47", "v48", "v49", "v50", "v51", "v52", "v53", "v54", "v55", "v56", "v57", "v58", "v59", "v60", "v61", "v62", "v63", "v64", "v65", "v66", "v67", "v68", "v69", "v70", "v71", "v72", "v73", "v74", "v75", "v76", "v77", "v78", "v79", "v80", "v81", "v82", "v83", "v84", "v85", "v86", "v87", "v88", "v89", "v90", "v91", "v92", "v93", "v94", "v95", "v96", "v97", "v98", "v99", "v100", "v101", "v102", "v103", "v104", "v105", "v106", "v107", "v108", "v109", "v110", "v111", "v112", "v113", "v114", "v115", "v116", "v117", "v118", "v119", "v120", "v121", "v122", "v123", "v124", "v125", "v126", "v127", "v128", "v129", "v130", "v131", "v132", "v133", "v134", "v135", "v136", "v137", "v138", "v139", "v140", "v141", "v142", "v143", "v144", "v145", "v146", "v147", "v148", "v149", "v150", "v151", "v152", "v153", "v154", "v155", "v156", "v157", "v158", "v159", "v160", "v161", "v162", "v163", "v164", "v165", "v166", "v167", "v168", "v169", "v170", "v171", "v172", "v173", "v174", "v175", "v176", "v177", "v178", "v179", "v180", "v181", "v182", "v183", "v184", "v185", "v186", "v187", "v188", "v189", "v190", "v191", "v192", "v193", "v194", "v195", "v196", "v197", "v198", "v199", "v200", "v201", "v202", "v203", "v204", "v205", "v206", "v207", "v208", "v209", "v210", "v211", "v212", "v213", "v214", "v215", "v216", "v217", "v218", "v219", "v220", "v221", "v222", "v223", "v224", "v225", "v226", "v227", "v228", "v229", "v230", "v231", "v232", "v233", "v234", "v235", "v236", "v237", "v238", "v239", "v240", "v241", "v242", "v243", "v244", "v245", "v246", "v247", "v248", "v249", "v250", "v251", "v252", "v253", "v254", "v255", "a0", "a1", "a2", "a3", "a4", "a5", "a6", "a7", "a8", "a9", "a10", "a11", "a12", "a13", "a14", "a15", "a16", "a17", "a18", "a19", "a20", "a21", "a22", "a23", "a24", "a25", "a26", "a27", "a28", "a29", "a30", "a31", "a32", "a33", "a34", "a35", "a36", "a37", "a38", "a39", "a40", "a41", "a42", "a43", "a44", "a45", "a46", "a47", "a48", "a49", "a50", "a51", "a52", "a53", "a54", "a55", "a56", "a57", "a58", "a59", "a60", "a61", "a62", "a63", "a64", "a65", "a66", "a67", "a68", "a69", "a70", "a71", "a72", "a73", "a74", "a75", "a76", "a77", "a78", "a79", "a80", "a81", "a82", "a83", "a84", "a85", "a86", "a87", "a88", "a89", "a90", "a91", "a92", "a93", "a94", "a95", "a96", "a97", "a98", "a99", "a100", "a101", "a102", "a103", "a104", "a105", "a106", "a107", "a108", "a109", "a110", "a111", "a112", "a113", "a114", "a115", "a116", "a117", "a118", "a119", "a120", "a121", "a122", "a123", "a124", "a125", "a126", "a127", "a128", "a129", "a130", "a131", "a132", "a133", "a134", "a135", "a136", "a137", "a138", "a139", "a140", "a141", "a142", "a143", "a144", "a145", "a146", "a147", "a148", "a149", "a150", "a151", "a152", "a153", "a154", "a155", "a156", "a157", "a158", "a159", "a160", "a161", "a162", "a163", "a164", "a165", "a166", "a167", "a168", "a169", "a170", "a171", "a172", "a173", "a174", "a175", "a176", "a177", "a178", "a179", "a180", "a181", "a182", "a183", "a184", "a185", "a186", "a187", "a188", "a189", "a190", "a191", "a192", "a193", "a194", "a195", "a196", "a197", "a198", "a199", "a200", "a201", "a202", "a203", "a204", "a205", "a206", "a207", "a208", "a209", "a210", "a211", "a212", "a213", "a214", "a215", "a216", "a217", "a218", "a219", "a220", "a221", "a222", "a223", "a224", "a225", "a226", "a227", "a228", "a229", "a230", "a231", "a232", "a233", "a234", "a235", "a236", "a237", "a238", "a239", "a240", "a241", "a242", "a243", "a244", "a245", "a246", "a247", "a248", "a249", "a250", "a251", "a252", "a253", "a254", "a255", "s3", "s4", "s5", "s6", "s7", "s8", "s9", "s10", "s11", "s12", "s13", "s14", "s15", "s16", "s17", "s18", "s19", "s20", "s21", "s22", "s23", "s24", "s25", "s26", "s27", "s28", "s29", "s30", "s31", "s33", "s34", "s35", "s36", "s37", "s38", "s39", "s40", "s41", "s42", "s43", "s44", "s45", "s46", "s47", "s48", "s49", "s50", "s51", "s52", "s53", "s54", "s55", "s56", "s57", "s58", "s59", "s60", "s61", "s62", "s63", "s64", "s65", "s66", "s67", "s68", "s69", "s70", "s71", "s72", "s73", "s74", "s75", "s76", "s77", "s78", "s79", "s80", "s81", "s82", "s83", "s84", "s85", "s86", "s87", "s88", "s89", "s90", "s91", "s92", "s93", "s94", "s95", "s96", "s97", "s98", "s99", "vcc", "scc", "memory");
}

extern "C" void kernel_launch(void* const* d_in, const int* in_sizes, int n_in, void* d_out, int out_size, void* d_ws, size_t ws_size,
                              hipStream_t stream) {
  (void)hipFuncSetAttribute((const void*)block_fwd, hipFuncAttributeMaxDynamicSharedMemorySize, 160 * 1024);
  block_fwd<<<256, 256, 160 * 1024, stream>>>(d_in[0], d_in[1], d_in[2], d_in[4], d_in[5], d_in[6], d_in[7], d_out, d_ws);
}
```
